```python
import torch

def _softmax(x, axis=-1):
    return torch.softmax(x, dim=axis)
def _gelu(x):
    return torch.nn.functional.gelu(x, approximate="tanh")
def _silu(x):
    return torch.nn.functional.silu(x)
def _sigmoid(x):
    return torch.sigmoid(x)
def _layernorm(x, axis=-1, eps=1e-5):
    m = x.mean(dim=axis, keepdim=True)
    v = x.var(dim=axis, keepdim=True, unbiased=False)
    return (x - m) / torch.sqrt(v + eps)
def _scatter_add(tgt, idx, src, axis=0):
    return tgt.clone().index_add_(axis, idx, src)
def _segment_sum(src, seg, n):
    out = torch.zeros((n,) + src.shape[1:], dtype=src.dtype)
    return out.index_add_(0, seg, src)
def _pad(x, pw):
    flat = tuple(v for pair in reversed(pw) for v in pair)
    return torch.nn.functional.pad(x, flat)

kernel_name = 'sequential_preLN__mha_block_cached__geglu_ffn__decode'

def setup_inputs():

    _w = (lambda a: (lambda t: t.to(torch.bfloat16) if t.is_floating_point() else t)(torch.as_tensor(a)))
    def _A_setup():
        B, Sq, D = 2, 1, 512
        H, Sk, Dh = 32, 8192, 128
        return {
            "x": _w((lambda *s: torch.randn(*s, dtype=torch.bfloat16) * 0.05)(B, Sq, D)),
            "k_cache": _w((lambda *s: torch.randn(*s, dtype=torch.bfloat16) * 3.2)(B, H, Sk, Dh)),
            "v_cache": _w((lambda *s: torch.randn(*s, dtype=torch.bfloat16) * 0.4)(B, H, Sk, Dh)),
            "wq": _w((lambda *s: torch.randn(*s, dtype=torch.bfloat16) * 3.2)(D, H * Dh) * (D ** -0.5)),
            "wo": _w((lambda *s: torch.randn(*s, dtype=torch.bfloat16) * 0.4)(H * Dh, D) * ((H * Dh) ** -0.5)),
        }
    _A_in = _A_setup()

    def _B_setup():
        B, S, D, F = 2, 1, 512, 1376
        return {
            "x": _w((lambda *s: torch.randn(*s, dtype=torch.bfloat16) * 0.05)(B, S, D)),
            "w_gate": _w((lambda *s: torch.randn(*s, dtype=torch.bfloat16) * 0.05)(D, F)),
            "w_up": _w((lambda *s: torch.randn(*s, dtype=torch.bfloat16) * 0.05)(D, F)),
            "w_down": _w((lambda *s: torch.randn(*s, dtype=torch.bfloat16) * 0.05)(F, D)),
        }
    _B_in = _B_setup()

    return {
        "A_x": _A_in["x"],
        "A_k_cache": _A_in["k_cache"],
        "A_v_cache": _A_in["v_cache"],
        "A_wq": _A_in["wq"],
        "A_wo": _A_in["wo"],
        "B_x": _B_in["x"],
        "B_w_gate": _B_in["w_gate"],
        "B_w_up": _B_in["w_up"],
        "B_w_down": _B_in["w_down"],
    }

def reference(A_x, A_k_cache, A_v_cache, A_wq, A_wo, B_x, B_w_gate, B_w_up, B_w_down):
    def _A_ref(x, k_cache, v_cache, wq, wo):
        B, Sq, D = x.shape
        H, Dh = 32, 128
        q = (lambda x, *d: x.permute(*d))((x @ wq).reshape(B, Sq, H, Dh), 0, 2, 1, 3)
        scores = torch.einsum("bhqd,bhkd->bhqk", q, k_cache) * (Dh ** -0.5)
        p = _softmax(scores, axis=-1)
        o = torch.einsum("bhqk,bhkd->bhqd", p, v_cache)
        return (lambda x, *d: x.permute(*d))(o, 0, 2, 1, 3).reshape(B, Sq, H * Dh) @ wo

    def _B_ref(x, w_gate, w_up, w_down):
        return (_gelu(x @ w_gate) * (x @ w_up)) @ w_down

    x = A_x
    h = x + _A_ref(_layernorm(x, axis=-1), A_k_cache, A_v_cache, A_wq, A_wo)
    h2 = h + _B_ref(_layernorm(h, axis=-1), B_w_gate, B_w_up, B_w_down)
    return h2
```

```cpp
#include <hip/hip_runtime.h>
#include <hip/hip_bf16.h>
#include <stdint.h>

#define MIMIC 1

typedef unsigned u32x4 __attribute__((ext_vector_type(4)));
typedef float f32x4 __attribute__((ext_vector_type(4)));
typedef __bf16 bf16x2 __attribute__((ext_vector_type(2)));

constexpr int B = 2, D = 512, H = 32, SK = 8192, DH = 128, F = 1376;
constexpr int NSPLIT = 4;

__device__ __forceinline__ float bf2f(unsigned short x) { return __uint_as_float(((unsigned)x) << 16); }
__device__ __forceinline__ float rbf(float x) {
  unsigned u = __float_as_uint(x);
  u += 0x7FFFu + ((u >> 16) & 1u);
  return __uint_as_float(u & 0xFFFF0000u);
}
__device__ __forceinline__ float rb(float x) { return MIMIC ? rbf(x) : x; }
__device__ __forceinline__ unsigned short f2bf(float x) {
  unsigned u = __float_as_uint(x);
  u += 0x7FFFu + ((u >> 16) & 1u);
  return (unsigned short)(u >> 16);
}
__device__ __forceinline__ float dot2(unsigned k, unsigned q, float acc) {
  return __builtin_amdgcn_fdot2_f32_bf16(__builtin_bit_cast(bf16x2, k), __builtin_bit_cast(bf16x2, q), acc, false);
}
__device__ __forceinline__ float wave_sum(float x) {
  for (int m = 32; m >= 1; m >>= 1) x += __shfl_xor(x, m);
  return x;
}
__device__ __forceinline__ float wave_max(float x) {
  for (int m = 32; m >= 1; m >>= 1) x = fmaxf(x, __shfl_xor(x, m));
  return x;
}

__device__ float block_ln(float x, float* red) {
  const int tid = threadIdx.x, lane = tid & 63, w = tid >> 6;
  float s = wave_sum(x);
  if (lane == 0) red[w] = s;
  __syncthreads();
  float t = 0;
  for (int i = 0; i < 8; ++i) t += red[i];
  float meanf = t * (1.0f / D);
  __syncthreads();
  float d = x - meanf;
  float s2 = wave_sum(d * d);
  if (lane == 0) red[w] = s2;
  __syncthreads();
  float t2 = 0;
  for (int i = 0; i < 8; ++i) t2 += red[i];
  __syncthreads();
  float mean = rb(meanf);
  float var = rb(t2 * (1.0f / D));
  float num = rb(x - mean);
  float den = rb(sqrtf(rb(var + 1e-5f)));
  return rb(num / den);
}

__global__ __launch_bounds__(512) void k_attn(const unsigned short* x, const unsigned short* kc, const unsigned short* vc,
                                              const unsigned short* wq, float* part  ) {
  __shared__ float xn[D];
  __shared__ float red[8];
  __shared__ float qpart[4][DH];
  __shared__ unsigned qpk[DH / 2];
  __shared__ float sc[2048];
  __shared__ float opart[8][4][DH];
  __shared__ float wm[8], wl[8];
  const int tid = threadIdx.x, lane = tid & 63, w = tid >> 6;
  const int bid = blockIdx.x;
  const int xcd = bid & 7, j = bid >> 3;
  const int h = xcd * 4 + (j >> 3), b = (j >> 2) & 1, s = j & 3;
  float xv = bf2f(x[b * D + tid]);
  float n = block_ln(xv, red);
  xn[tid] = n;
  __syncthreads();
  {
    const int c = tid & 127, kq = tid >> 7;
    float acc = 0;
    for (int k = kq * 128; k < kq * 128 + 128; ++k) acc += xn[k] * bf2f(wq[(size_t)k * (H * DH) + h * DH + c]);
    qpart[kq][c] = acc;
  }
  __syncthreads();
  if (tid < 64) {
    float q0 = qpart[0][2 * tid] + qpart[1][2 * tid] + qpart[2][2 * tid] + qpart[3][2 * tid];
    float q1 = qpart[0][2 * tid + 1] + qpart[1][2 * tid + 1] + qpart[2][2 * tid + 1] + qpart[3][2 * tid + 1];
    qpk[tid] = (unsigned)f2bf(q0) | ((unsigned)f2bf(q1) << 16);
  }
  __syncthreads();
  const int r = lane >> 4, c = lane & 15;
  u32x4 q4 = *(const u32x4*)&qpk[c * 4];
  const size_t base = ((size_t)(b * H + h) * SK + s * 2048 + w * 256) * DH;
  const u32x4* kp = (const u32x4*)(kc + base) + lane;
  const u32x4* vp = (const u32x4*)(vc + base) + lane;
  float* wsc = sc + w * 256;
  for (int i = 0; i < 64; i += 8) {
    u32x4 kk[8];
#pragma unroll
    for (int t = 0; t < 8; ++t) kk[t] = __builtin_nontemporal_load(kp + (i + t) * 64);
#pragma unroll
    for (int t = 0; t < 8; ++t) {
      float d = 0;
      d = dot2(kk[t][0], q4[0], d); d = dot2(kk[t][1], q4[1], d); d = dot2(kk[t][2], q4[2], d); d = dot2(kk[t][3], q4[3], d);
      d += __shfl_xor(d, 1); d += __shfl_xor(d, 2); d += __shfl_xor(d, 4); d += __shfl_xor(d, 8);
      if (c == 0) wsc[(i + t) * 4 + r] = rb(rb(d) * 0.08838834764831845f);
    }
  }
  __syncthreads();
  float m = -INFINITY;
  float sv[4];
  for (int i = 0; i < 4; ++i) { sv[i] = wsc[lane + 64 * i]; m = fmaxf(m, sv[i]); }
  m = wave_max(m);
  float l = 0;
  for (int i = 0; i < 4; ++i) { sv[i] = __expf(sv[i] - m); l += sv[i]; wsc[lane + 64 * i] = sv[i]; }
  l = wave_sum(l);
  __syncthreads();
  float acc[8] = {0, 0, 0, 0, 0, 0, 0, 0};
  for (int i = 0; i < 64; i += 8) {
    u32x4 vv[8];
#pragma unroll
    for (int t = 0; t < 8; ++t) vv[t] = __builtin_nontemporal_load(vp + (i + t) * 64);
#pragma unroll
    for (int t = 0; t < 8; ++t) {
      float p = wsc[(i + t) * 4 + r];
#pragma unroll
      for (int k = 0; k < 4; ++k) {
        acc[2 * k] += p * __uint_as_float(vv[t][k] << 16);
        acc[2 * k + 1] += p * __uint_as_float(vv[t][k] & 0xffff0000u);
      }
    }
  }
  for (int k = 0; k < 8; ++k) opart[w][r][c * 8 + k] = acc[k];
  if (lane == 0) { wm[w] = m; wl[w] = l; }
  __syncthreads();
  if (tid < 128) {
    float M = -INFINITY;
    for (int i = 0; i < 8; ++i) M = fmaxf(M, wm[i]);
    float L = 0, o = 0;
    for (int i = 0; i < 8; ++i) {
      float e = __expf(wm[i] - M);
      L += e * wl[i];
      o += e * (opart[i][0][tid] + opart[i][1][tid] + opart[i][2][tid] + opart[i][3][tid]);
    }
    float* pp = part + (size_t)((b * H + h) * NSPLIT + s) * 132;
    pp[tid] = o;
    if (tid == 0) { pp[128] = M; pp[129] = L; }
  }
}

__global__ __launch_bounds__(512) void k_wo(const float* part, const unsigned short* wo, float* y) {
  __shared__ float o[DH];
  const int tid = threadIdx.x;
  const int bh = blockIdx.x, h = bh & 31;
  if (tid < 128) {
    const float* pp = part + (size_t)bh * NSPLIT * 132;
    float M = -INFINITY;
    for (int s = 0; s < NSPLIT; ++s) M = fmaxf(M, pp[s * 132 + 128]);
    float L = 0, ov = 0;
    for (int s = 0; s < NSPLIT; ++s) {
      float e = __expf(pp[s * 132 + 128] - M);
      L += e * pp[s * 132 + 129];
      ov += e * pp[s * 132 + tid];
    }
    o[tid] = rb(ov / L);
  }
  __syncthreads();
  float acc = 0;
  for (int d = 0; d < DH; ++d) acc += o[d] * bf2f(wo[(size_t)(h * DH + d) * D + tid]);
  y[(size_t)bh * D + tid] = acc;
}

__global__ __launch_bounds__(512) void k_ffn(const unsigned short* x, const float* y, const unsigned short* wg, const unsigned short* wu,
                                             const unsigned short* wd, float* hbuf, float* zpart) {
  __shared__ float xn[B][D];
  __shared__ float red[8];
  __shared__ float gp[B][2][16][32];
  __shared__ float act[B][32];
  const int tid = threadIdx.x;
  const int j = blockIdx.x;
  for (int b = 0; b < B; ++b) {
    float a = 0;
    for (int h = 0; h < H; ++h) a += y[(size_t)(b * H + h) * D + tid];
    float hv = rb(bf2f(x[b * D + tid]) + rb(a));
    if (j == 0) hbuf[b * D + tid] = hv;
    xn[b][tid] = block_ln(hv, red);
  }
  __syncthreads();
  {
    const int c = tid & 31, kq = tid >> 5;
    float g0 = 0, g1 = 0, u0 = 0, u1 = 0;
    for (int k = kq * 32; k < kq * 32 + 32; ++k) {
      float wgv = bf2f(wg[(size_t)k * F + j * 32 + c]), wuv = bf2f(wu[(size_t)k * F + j * 32 + c]);
      g0 += xn[0][k] * wgv; g1 += xn[1][k] * wgv; u0 += xn[0][k] * wuv; u1 += xn[1][k] * wuv;
    }
    gp[0][0][kq][c] = g0; gp[1][0][kq][c] = g1; gp[0][1][kq][c] = u0; gp[1][1][kq][c] = u1;
  }
  __syncthreads();
  if (tid < 64) {
    const int b = tid >> 5, c = tid & 31;
    float g = 0, u = 0;
    for (int i = 0; i < 16; ++i) { g += gp[b][0][i][c]; u += gp[b][1][i][c]; }
    g = rb(g); u = rb(u);
    float t = tanhf(0.7978845608028654f * (g + 0.044715f * g * g * g));
    float ge = rb(0.5f * g * (1.0f + t));
    act[b][c] = rb(ge * u);
  }
  __syncthreads();
  float z0 = 0, z1 = 0;
  for (int c = 0; c < 32; ++c) {
    float wv = bf2f(wd[(size_t)(j * 32 + c) * D + tid]);
    z0 += act[0][c] * wv; z1 += act[1][c] * wv;
  }
  zpart[(size_t)(j * 2 + 0) * D + tid] = z0;
  zpart[(size_t)(j * 2 + 1) * D + tid] = z1;
}

__global__ __launch_bounds__(512) void k_fin(const float* hbuf, const float* zpart, unsigned short* out) {
  const int tid = threadIdx.x, b = blockIdx.x;
  float a = 0;
  for (int j = 0; j < 43; ++j) a += zpart[(size_t)(j * 2 + b) * D + tid];
  out[b * D + tid] = f2bf(hbuf[b * D + tid] + rb(a));
}

extern "C" void kernel_launch(void* const* d_in, const int* in_sizes, int n_in, void* d_out, int out_size, void* d_ws, size_t ws_size,
                              hipStream_t stream) {
  const unsigned short* x = (const unsigned short*)d_in[0];
  const unsigned short* kc = (const unsigned short*)d_in[1];
  const unsigned short* vc = (const unsigned short*)d_in[2];
  const unsigned short* wq = (const unsigned short*)d_in[3];
  const unsigned short* wo = (const unsigned short*)d_in[4];
  const unsigned short* wg = (const unsigned short*)d_in[6];
  const unsigned short* wu = (const unsigned short*)d_in[7];
  const unsigned short* wd = (const unsigned short*)d_in[8];
  float* ws = (float*)d_ws;
  float* part = ws;
  float* y = ws + 65536;
  float* hbuf = ws + 65536 * 2;
  float* zpart = ws + 65536 * 3;
  k_attn<<<256, 512, 0, stream>>>(x, kc, vc, wq, part);
  k_wo<<<64, 512, 0, stream>>>(part, wo, y);
  k_ffn<<<43, 512, 0, stream>>>(x, y, wg, wu, wd, hbuf, zpart);
  k_fin<<<2, 512, 0, stream>>>(hbuf, zpart, (unsigned short*)d_out);
}
```

```cpp
#include <hip/hip_runtime.h>
#include <stdint.h>

#define DBG 0
#define TAU 0
#define TOFS 0
#define STEPBAR 0

typedef unsigned u32x4 __attribute__((ext_vector_type(4)));
typedef unsigned u32x2 __attribute__((ext_vector_type(2)));
typedef float f32x4 __attribute__((ext_vector_type(4)));
typedef float f32x2 __attribute__((ext_vector_type(2)));
typedef __bf16 bf16x2 __attribute__((ext_vector_type(2)));
typedef __bf16 bf16x8 __attribute__((ext_vector_type(8)));
typedef short s16x4 __attribute__((ext_vector_type(4)));
typedef unsigned long long u64;
#define GAS __attribute__((address_space(1)))
#define LAS __attribute__((address_space(3)))
#define CAS __attribute__((address_space(4)))
typedef GAS u64 gu64;
typedef GAS unsigned gu32;
#define RLX_AGENT __ATOMIC_RELAXED, __HIP_MEMORY_SCOPE_AGENT

constexpr int D = 512, H = 32, SK = 8192, DH = 128, F = 1376;
constexpr int NWG = 256, NT = 320, NFFN = 86;
constexpr int REP_L = 16, REP_H = 8;
constexpr unsigned WS_W = 0;
constexpr unsigned WS_TK = 1024;
constexpr unsigned WS_LG = 4096;
constexpr unsigned WS_HG = WS_LG + 32768;
constexpr unsigned WS_QG = WS_HG + 32768;
constexpr unsigned WS_YG = 131072;
constexpr unsigned WS_ZG = WS_YG + (1u << 20);
constexpr unsigned WS_USED = WS_ZG + 256 * 384 * 8;
constexpr size_t WS_DBG = (size_t)256 << 20;
constexpr unsigned SPIN_CAP = 1u << 16;
constexpr int AUX_SC1 = 16;

struct Params {
  const unsigned short* x; const unsigned short* kc; const unsigned short* vc; const unsigned short* wq; const unsigned short* wo;
  const unsigned short* wg; const unsigned short* wu; const unsigned short* wd;
  unsigned short* out; unsigned char* ws;
};

__device__ __forceinline__ float dot2(unsigned a, unsigned b, float acc) {
  return __builtin_amdgcn_fdot2_f32_bf16(__builtin_bit_cast(bf16x2, a), __builtin_bit_cast(bf16x2, b), acc, false);
}
__device__ __forceinline__ unsigned cvtpk(float lo, float hi) {
  const f32x2 f = {lo, hi};
  return __builtin_bit_cast(unsigned, __builtin_convertvector(f, bf16x2));
}
__device__ __forceinline__ float lo2f(unsigned u) { return __uint_as_float(u << 16); }
__device__ __forceinline__ float hi2f(unsigned u) { return __uint_as_float(u & 0xffff0000u); }
__device__ __forceinline__ float rbf(float x) { return lo2f(cvtpk(x, x)); }
template <int CTRL> __device__ __forceinline__ float dpp(float x) {
  return __builtin_bit_cast(float, __builtin_amdgcn_mov_dpp(__builtin_bit_cast(int, x), CTRL, 0xf, 0xf, true));
}
template <int CTRL> __device__ __forceinline__ unsigned dppu(unsigned x) {
  return (unsigned)__builtin_amdgcn_mov_dpp((int)x, CTRL, 0xf, 0xf, true);
}
constexpr int ROR1 = 0x121, ROR2 = 0x122, ROR4 = 0x124, ROR8 = 0x128, XOR1 = 0xB1, XOR2 = 0x4E, XOR7 = 0x141;
__device__ __forceinline__ float x16_sum(float x) {
  auto s = __builtin_amdgcn_permlane16_swap(__float_as_uint(x), __float_as_uint(x), false, false);
  return __uint_as_float(s[0]) + __uint_as_float(s[1]);
}
__device__ __forceinline__ float x32_sum(float x) {
  auto s = __builtin_amdgcn_permlane32_swap(__float_as_uint(x), __float_as_uint(x), false, false);
  return __uint_as_float(s[0]) + __uint_as_float(s[1]);
}
__device__ __forceinline__ float x16_max(float x) {
  auto s = __builtin_amdgcn_permlane16_swap(__float_as_uint(x), __float_as_uint(x), false, false);
  return fmaxf(__uint_as_float(s[0]), __uint_as_float(s[1]));
}
__device__ __forceinline__ float x32_max(float x) {
  auto s = __builtin_amdgcn_permlane32_swap(__float_as_uint(x), __float_as_uint(x), false, false);
  return fmaxf(__uint_as_float(s[0]), __uint_as_float(s[1]));
}
__device__ __forceinline__ float rs32(float a, float b) {
  auto s = __builtin_amdgcn_permlane32_swap(__float_as_uint(a), __float_as_uint(b), false, false);
  return __uint_as_float(s[0]) + __uint_as_float(s[1]);
}
__device__ __forceinline__ float rs16(float a, float b) {
  auto s = __builtin_amdgcn_permlane16_swap(__float_as_uint(a), __float_as_uint(b), false, false);
  return __uint_as_float(s[0]) + __uint_as_float(s[1]);
}
__device__ __forceinline__ float row_sum16(float x) {
  x += dpp<ROR8>(x); x += dpp<ROR4>(x); x += dpp<ROR2>(x); x += dpp<ROR1>(x); return x;
}
__device__ __forceinline__ unsigned perm_lo(unsigned a, unsigned b) { return __builtin_amdgcn_perm(b, a, 0x05040100u); }
__device__ __forceinline__ unsigned perm_hi(unsigned a, unsigned b) { return __builtin_amdgcn_perm(b, a, 0x07060302u); }
__device__ __forceinline__ u32x4 ldnt(const void* p) { return __builtin_nontemporal_load((const GAS u32x4*)p); }
__device__ __forceinline__ u32x4 ldpl(const void* p) { return *(const GAS u32x4*)p; }
__device__ __forceinline__ unsigned mix32(unsigned h) {
  h ^= h >> 16; h *= 0x85ebca6bu; h ^= h >> 13; h *= 0xc2b2ae35u; h ^= h >> 16; return h;
}
__device__ __forceinline__ float frcp(float x) { return __builtin_amdgcn_rcpf(x); }
__device__ __forceinline__ float ex2(float x) { return __builtin_amdgcn_exp2f(x); }
#define SB() __builtin_amdgcn_sched_barrier(0)
#define CFENCE() asm volatile("" ::: "memory")
#define PIN2(a, b) asm volatile("" : "+v"(a), "+v"(b) :: "memory")
#define PIN4(a) asm volatile("" : "+v"(a[0]), "+v"(a[1]), "+v"(a[2]), "+v"(a[3]) :: "memory")
#define PIN8(a) asm volatile("" : "+v"(a[0]), "+v"(a[1]), "+v"(a[2]), "+v"(a[3]), "+v"(a[4]), "+v"(a[5]), "+v"(a[6]), "+v"(a[7]) :: "memory")
#define STAMP(i) do {} while (0)
#define DUMP() do {} while (0)
#define ST16(v, off) __builtin_amdgcn_raw_buffer_store_b128((v), rs, (int)(off), 0, AUX_SC1)
#define ST8(v, off) __builtin_amdgcn_raw_buffer_store_b64((v), rs, (int)(off), 0, AUX_SC1)
#define LD16(off) __builtin_amdgcn_raw_buffer_load_b128(rs, (int)(off), 0, AUX_SC1)
#define LD8(off) __builtin_amdgcn_raw_buffer_load_b64(rs, (int)(off), 0, AUX_SC1)

constexpr float LOG2E = 1.4426950408889634f;
constexpr float SCALE = 0.08838834764831845f;

__device__ __forceinline__ void ln_consts(float mean, float var, float& mb, float& r) {
  mb = rbf(mean);
  r = frcp(rbf(__builtin_amdgcn_sqrtf(rbf(rbf(var) + 1e-5f))));
}
__device__ __forceinline__ unsigned ln_pair(unsigned raw, float mb, float r) {
  const unsigned n = cvtpk(lo2f(raw) - mb, hi2f(raw) - mb);
  return cvtpk(lo2f(n) * r, hi2f(n) * r);
}

constexpr int L_BIG = 0;
constexpr int L_IMG = 0;
constexpr int L_XN = 131072;
constexpr int L_Q = L_XN + 2048;
constexpr int L_OW = L_Q + 256;
constexpr int L_ML = L_OW + 2048;
constexpr int L_OP = L_ML + 64;
constexpr int L_ST = L_OP + 1024;
constexpr int L_GP = L_ST + 64;
constexpr int L_ROLE = L_GP + 512;
constexpr int LDS_BYTES = L_ROLE + 64;

struct Tile { u32x4 k[4]; u32x4 v[4]; };

__global__ __launch_bounds__(NT, 2) void fused_kernel(Params p) {
  extern __shared__ __attribute__((aligned(16))) unsigned char lds[];
  const int tid = threadIdx.x, lane = tid & 63, wv = __builtin_amdgcn_readfirstlane(tid >> 6), w = wv - 1;
  const int bid = blockIdx.x;
  STAMP(0);
  const int xcd = bid & 7, jj = bid >> 3;
  const int h = xcd * 4 + (jj >> 3), b = (jj >> 2) & 1, sp = jj & 3;
  const int lid = (b * H + h) * 4 + sp;
  const int r = lane >> 4, c = lane & 15;
  const __amdgpu_buffer_rsrc_t rs = __builtin_amdgcn_make_buffer_rsrc((void*)p.ws, 0, (int)WS_USED, 0x00020000);
  const unsigned wcnt = *(const CAS unsigned*)(size_t)(p.ws + WS_W);
  if (wv == 0) {
    __builtin_amdgcn_s_setprio(3);
    const u32x4 xraw = ldpl(p.x + b * D + lane * 8);
    u32x4 wq[32];
    const unsigned char* q_ = (const unsigned char*)p.wq + ((size_t)(32 * (lane >> 2)) * (H * DH) + h * DH + 32 * sp + 8 * (lane & 3)) * 2;
#pragma unroll
    for (int i = 0; i < 32; ++i) wq[i] = ldnt(q_ + (size_t)i * (H * DH * 2));
    CFENCE(); SB();
    __builtin_amdgcn_s_barrier();
    SB();
    const unsigned E = mix32(wcnt + 1u);
    if (lane == 0) {
      *(volatile LAS unsigned*)(size_t)(unsigned)L_ROLE = 0u;
      if (bid == 0) ST8((u32x2{0u, E}), WS_TK);
    }
    float xv[8];
#pragma unroll
    for (int i = 0; i < 4; ++i) { xv[2 * i] = lo2f(xraw[i]); xv[2 * i + 1] = hi2f(xraw[i]); }
    const float c0 = __uint_as_float(__builtin_amdgcn_readfirstlane(__float_as_uint(xv[0])));
    float s = 0.f, q = 0.f;
#pragma unroll
    for (int i = 0; i < 8; ++i) { const float d = xv[i] - c0; s += d; q = fmaf(d, d, q); }
    s = row_sum16(s); q = row_sum16(q);
    s = x32_sum(x16_sum(s)); q = x32_sum(x16_sum(q));
    const float ms = s * (1.0f / D);
    float mb, rr;
    ln_consts(c0 + ms, fmaxf(q * (1.0f / D) - ms * ms, 0.f), mb, rr);
    u32x4* xs = (u32x4*)(lds + L_XN);
    xs[lane] = u32x4{ln_pair(xraw[0], mb, rr), ln_pair(xraw[1], mb, rr), ln_pair(xraw[2], mb, rr), ln_pair(xraw[3], mb, rr)};
    SB();
#pragma unroll
    for (int i = 0; i < 32; i += 8)
      asm volatile("" : "+v"(wq[i]), "+v"(wq[i + 1]), "+v"(wq[i + 2]), "+v"(wq[i + 3]), "+v"(wq[i + 4]), "+v"(wq[i + 5]), "+v"(wq[i + 6]), "+v"(wq[i + 7]));
    STAMP(1);
    u32x4 xp[4];
#pragma unroll
    for (int i = 0; i < 4; ++i) xp[i] = xs[(lane & ~3) + i];
    float acc[8] = {0, 0, 0, 0, 0, 0, 0, 0};
#pragma unroll
    for (int pp = 0; pp < 16; ++pp)
#pragma unroll
      for (int k = 0; k < 4; ++k) {
        acc[2 * k] = dot2(perm_lo(wq[2 * pp][k], wq[2 * pp + 1][k]), xp[pp >> 2][pp & 3], acc[2 * k]);
        acc[2 * k + 1] = dot2(perm_hi(wq[2 * pp][k], wq[2 * pp + 1][k]), xp[pp >> 2][pp & 3], acc[2 * k + 1]);
      }
    const float t0 = rs32(acc[0], acc[4]), t1 = rs32(acc[1], acc[5]), t2 = rs32(acc[2], acc[6]), t3 = rs32(acc[3], acc[7]);
    float u0 = rs16(t0, t2), u1 = rs16(t1, t3);
    u0 += dpp<ROR8>(u0); u1 += dpp<ROR8>(u1); u0 += dpp<ROR4>(u0); u1 += dpp<ROR4>(u1);
    const unsigned qg = WS_QG + (unsigned)((b * H + h) * 64) * 8;
    if ((lane & 12) == 0) ST8((u32x2{cvtpk(u0, u1), E}), qg + (16 * sp + 4 * (lane & 3) + 2 * (lane >> 5) + ((lane >> 4) & 1)) * 8);
    STAMP(2);
    u32x2 g;
    for (unsigned spins = 0; spins < SPIN_CAP; ++spins) {
      CFENCE();
      g = LD8(qg + lane * 8);
      if (__all(g[1] == E)) break;
    }
    ((unsigned*)(lds + L_Q))[lane] = g[0];
    __syncthreads();
    STAMP(3);
    return;
  }
  const int rot = (lid & 7) * 4;
  const unsigned char* kbase = (const unsigned char*)p.kc + ((size_t)lid * 2048 * DH + (size_t)w * 16 * DH) * 2 + lane * 16;
  const unsigned char* vbase = (const unsigned char*)p.vc + ((size_t)lid * 2048 * DH + (size_t)w * 16 * DH) * 2 + lane * 16;
#define TOFF(i) ((size_t)((((i) + rot) & 31)) * (4 * 16 * DH * 2))
#define LOADK(T, i) do { const unsigned char* q_ = kbase + TOFF(i); _Pragma("unroll") for (int t_ = 0; t_ < 4; ++t_) T.k[t_] = ldnt(q_ + t_ * 1024); CFENCE(); SB(); } while (0)
#define LOADV(T, i) do { const unsigned char* q_ = vbase + TOFF(i); _Pragma("unroll") for (int t_ = 0; t_ < 4; ++t_) T.v[t_] = ldnt(q_ + t_ * 1024); CFENCE(); SB(); } while (0)
  Tile T0, T1, T2, T3;
  SB();
  __builtin_amdgcn_s_barrier();
  SB();
#define PACE() asm volatile("s_waitcnt vmcnt(12)" ::: "memory")
  LOADK(T0, 0); LOADV(T0, 0); LOADK(T1, 1); LOADV(T1, 1); PACE(); LOADK(T2, 2); PACE(); LOADV(T2, 2); PACE(); LOADK(T3, 3); PACE(); LOADV(T3, 3);
  {
    unsigned keep;
    asm volatile("s_mov_b32 %0, m0" : "=s"(keep));
#pragma unroll
    for (int j = 0; j < 4; ++j) {
      const unsigned char* kp = kbase + TOFF(4 + j);
      const unsigned char* vp = vbase + TOFF(4 + j);
      PACE();
#pragma unroll
      for (int t = 0; t < 4; ++t)
        asm volatile("s_mov_b32 m0, %1\n\ts_nop 0\n\tglobal_load_lds_dwordx4 %0, off nt" :: "v"(kp + t * 1024), "s"(L_BIG + w * 32768 + j * 8192 + t * 1024) : "memory");
      PACE();
#pragma unroll
      for (int t = 0; t < 4; ++t)
        asm volatile("s_mov_b32 m0, %1\n\ts_nop 0\n\tglobal_load_lds_dwordx4 %0, off nt" :: "v"(vp + t * 1024), "s"(L_BIG + w * 32768 + j * 8192 + 4096 + t * 1024) : "memory");
    }
    asm volatile("s_mov_b32 m0, %0" :: "s"(keep));
  }
  const unsigned E = mix32(wcnt + 1u);
  __syncthreads();
  const u32x4 q4 = *(const u32x4*)(lds + L_Q + c * 16);
  STAMP(3);
  float m = -INFINITY, l = 0.f;
  float acc[8] = {0, 0, 0, 0, 0, 0, 0, 0};
  auto qk = [&](const Tile& T, float (&s)[4]) {
#pragma unroll
    for (int t = 0; t < 4; ++t) {
      float d = dot2(T.k[t][0], q4[0], 0.f);
      d = dot2(T.k[t][1], q4[1], d); d = dot2(T.k[t][2], q4[2], d); d = dot2(T.k[t][3], q4[3], d);
      s[t] = d;
    }
  };
  auto sm = [&](float (&s)[4]) {
#pragma unroll
    for (int t = 0; t < 4; ++t) s[t] = row_sum16(s[t]);
    const unsigned a01 = cvtpk(s[0], s[1]), a23 = cvtpk(s[2], s[3]);
    const unsigned b01 = cvtpk(lo2f(a01) * SCALE, hi2f(a01) * SCALE), b23 = cvtpk(lo2f(a23) * SCALE, hi2f(a23) * SCALE);
    const float z0 = lo2f(b01) * LOG2E, z1 = hi2f(b01) * LOG2E, z2 = lo2f(b23) * LOG2E, z3 = hi2f(b23) * LOG2E;
    const float mn = fmaxf(fmaxf(fmaxf(z0, z1), fmaxf(z2, z3)), m);
    const float al = ex2(m - mn);
    s[0] = ex2(z0 - mn); s[1] = ex2(z1 - mn); s[2] = ex2(z2 - mn); s[3] = ex2(z3 - mn);
    l = fmaf(l, al, (s[0] + s[1]) + (s[2] + s[3]));
    m = mn;
#pragma unroll
    for (int k = 0; k < 8; ++k) acc[k] *= al;
  };
  auto pv = [&](const Tile& T, const float (&s)[4]) {
#pragma unroll
    for (int t = 0; t < 4; ++t)
#pragma unroll
      for (int k = 0; k < 4; ++k) {
        acc[2 * k] = fmaf(s[t], lo2f(T.v[t][k]), acc[2 * k]);
        acc[2 * k + 1] = fmaf(s[t], hi2f(T.v[t][k]), acc[2 * k + 1]);
      }
  };
#define SBAR() do {} while (0)
#define WAITT(n) do {} while (0)
#define STEP(T, i_next) do { float s_[4]; qk(T, s_); PIN4(s_); SB(); WAITT(i_next); SB(); LOADK(T, i_next); sm(s_); pv(T, s_); PIN8(acc); SB(); LOADV(T, i_next); SBAR(); } while (0)
#define STEP_LAST(T) do { float s_[4]; qk(T, s_); sm(s_); pv(T, s_); PIN8(acc); SB(); } while (0)
  STEP(T0, 8); STEP(T1, 9); STEP(T2, 10); STEP(T3, 11);
  {
    const u32x4* tl = (const u32x4*)(lds + L_BIG + w * 32768) + lane;
#define STEP_LDS(j, cnt) do { asm volatile("s_waitcnt vmcnt(" #cnt ")" ::: "memory"); Tile TL; \
      _Pragma("unroll") for (int t_ = 0; t_ < 4; ++t_) { TL.k[t_] = tl[(j * 8 + t_) * 64]; TL.v[t_] = tl[(j * 8 + 4 + t_) * 64]; } \
      float s_[4]; qk(TL, s_); sm(s_); pv(TL, s_); PIN8(acc); SB(); SBAR(); } while (0)
    STEP_LDS(0, 56); STEP_LDS(1, 48); STEP_LDS(2, 40); STEP_LDS(3, 32);
  }
  {
    const unsigned char* wob = (const unsigned char*)p.wo + ((size_t)(h * DH + 32 * r) * D + 128 * w + 8 * c) * 2;
    u32x4* wl = (u32x4*)(lds + L_BIG + w * 32768) + lane;
    for (int it = 0; it < 5; ++it) {
      u32x4 wo[8];
      const int ch = it < 4 ? it : 3;
#pragma unroll
      for (int i = 0; i < 8; ++i) wo[i] = ldnt(wob + (size_t)(8 * ch + i) * (D * 2));
      CFENCE(); SB();
      STEP(T0, 4 * it + 12); STEP(T1, 4 * it + 13); STEP(T2, 4 * it + 14); STEP(T3, 4 * it + 15);
#pragma unroll
      for (int pp = 0; pp < 4; ++pp) {
        u32x4 lo, hi;
#pragma unroll
        for (int k = 0; k < 4; ++k) { lo[k] = perm_lo(wo[2 * pp][k], wo[2 * pp + 1][k]); hi[k] = perm_hi(wo[2 * pp][k], wo[2 * pp + 1][k]); }
        wl[(8 * ch + 2 * pp) * 64] = lo; wl[(8 * ch + 2 * pp + 1) * 64] = hi;
      }
      SB();
    }
  }
  const int rb = bid >> 7, cb = bid & 127;
  const int ycol = 128 * w + 8 * c + 4 * (r >> 1) + 2 * (r & 1);
  unsigned yoff = WS_YG + (unsigned)((b * 128 + (ycol >> 2)) * 512 + sp * 128 + h * 4 + (ycol & 3)) * 8;
  unsigned zoff = WS_ZG + (unsigned)((ycol >> 2) * 384 + (ycol & 3)) * 8;
  PIN2(yoff, zoff);
  const unsigned short xres_raw = p.x[rb * D + 4 * cb + 2 * (lane & 1) + (lane >> 5)];
  u32x4 wop[32];
  const u32x4* wlr = (const u32x4*)(lds + L_BIG + w * 32768) + lane;
  STEP_LAST(T0);
#pragma unroll
  for (int i = 0; i < 8; ++i) wop[i] = wlr[i * 64];
  SB();
  STEP_LAST(T1);
#pragma unroll
  for (int i = 8; i < 16; ++i) wop[i] = wlr[i * 64];
  SB();
  STEP_LAST(T2);
#pragma unroll
  for (int i = 16; i < 24; ++i) wop[i] = wlr[i * 64];
  SB();
  STEP_LAST(T3);
#pragma unroll
  for (int i = 24; i < 32; ++i) wop[i] = wlr[i * 64];
  STAMP(4);
  {
    const float mw = x32_max(x16_max(m));
    const float e = ex2(m - mw);
    const float lw = x32_sum(x16_sum(l * e));
    float a[8];
#pragma unroll
    for (int k = 0; k < 8; ++k) a[k] = acc[k] * e;
    const float t0 = rs32(a[0], a[4]), t1 = rs32(a[1], a[5]), t2 = rs32(a[2], a[6]), t3 = rs32(a[3], a[7]);
    const float u0 = rs16(t0, t2), u1 = rs16(t1, t3);
    float* ow = (float*)(lds + L_OW) + w * 128 + c * 8 + (r >> 1) * 4 + (r & 1) * 2;
    *(float2*)ow = make_float2(u0, u1);
    if (lane == 0) { ((float*)(lds + L_ML))[w] = mw; ((float*)(lds + L_ML))[4 + w] = lw; }
  }
  STAMP(5);
  __syncthreads();
  STAMP(6);
  {
    const f32x4 m4 = *(const f32x4*)(lds + L_ML), l4 = *(const f32x4*)(lds + L_ML + 16);
    const float M = fmaxf(fmaxf(m4[0], m4[1]), fmaxf(m4[2], m4[3]));
    float L = 0.f, o0 = 0.f, o1 = 0.f;
#pragma unroll
    for (int ww = 0; ww < 4; ++ww) {
      const float e = ex2(m4[ww] - M);
      L = fmaf(e, l4[ww], L);
      const float2 v = *(const float2*)((const float*)(lds + L_OW) + ww * 128 + 2 * lane);
      o0 = fmaf(e, v.x, o0); o1 = fmaf(e, v.y, o1);
    }
    const float inv = frcp(L);
    unsigned* op = (unsigned*)(lds + L_OP) + w * 64;
    op[lane] = cvtpk(o0 * inv, o1 * inv);
    if (w == 0 && lane < REP_L)
      ST8((u32x2{__float_as_uint(M + __builtin_amdgcn_logf(L)), E}), WS_LG + (lane * 256 + b * 128 + h * 4 + sp) * 8);
    u32x4 ov[4];
#pragma unroll
    for (int i = 0; i < 4; ++i) ov[i] = *(const u32x4*)(op + 16 * r + 4 * i);
    float y[8] = {0, 0, 0, 0, 0, 0, 0, 0};
#pragma unroll
    for (int pp = 0; pp < 16; ++pp) {
      const unsigned o2 = ov[pp >> 2][pp & 3];
#pragma unroll
      for (int k = 0; k < 4; ++k) {
        y[2 * k] = dot2(wop[2 * pp][k], o2, y[2 * k]);
        y[2 * k + 1] = dot2(wop[2 * pp + 1][k], o2, y[2 * k + 1]);
      }
    }
    const float t0 = rs32(y[0], y[4]), t1 = rs32(y[1], y[5]), t2 = rs32(y[2], y[6]), t3 = rs32(y[3], y[7]);
    const float u0 = rs16(t0, t2), u1 = rs16(t1, t3);
    ST16((u32x4{__float_as_uint(u0), E, __float_as_uint(u1), E}), yoff);
  }
  STAMP(7);
  volatile LAS unsigned* role = (volatile LAS unsigned*)(size_t)(unsigned)L_ROLE;
  int fj = NFFN;
  if (w != 0) {
    if (w == 1) {
      unsigned tk = 0;
      if (lane == 0) {
        for (unsigned spins = 0; spins < SPIN_CAP; ++spins) {
          const u64 old = __hip_atomic_fetch_add((gu64*)(p.ws + WS_TK), 1ull, RLX_AGENT);
          tk = (unsigned)old;
          if ((unsigned)(old >> 32) == E) break;
          tk = NFFN;
          for (; spins < SPIN_CAP && (unsigned)(__hip_atomic_load((gu64*)(p.ws + WS_TK), RLX_AGENT) >> 32) != E; ++spins) __builtin_amdgcn_s_sleep(2);
        }
        *role = tk + 1u;
      }
    }
    unsigned rv;
    for (unsigned spins = 0; spins < (SPIN_CAP << 4); ++spins) { rv = *role; if (rv != 0u) break; __builtin_amdgcn_s_sleep(1); }
    fj = __builtin_amdgcn_readfirstlane(rv) - 1;
    if (fj >= NFFN) { DUMP(); return; }
    {
      const unsigned short* src = p.wg + (size_t)(lane >> 1) * F + 16 * fj + 8 * (lane & 1);
      const unsigned short* srcu = p.wu + (size_t)(lane >> 1) * F + 16 * fj + 8 * (lane & 1);
      unsigned keep;
      asm volatile("s_mov_b32 %0, m0" : "=s"(keep));
      for (int i = w - 1; i < 32; i += 3) {
        const unsigned short* g = (i < 16 ? src : srcu) + (size_t)(i & 15) * 32 * F;
        asm volatile("s_mov_b32 m0, %1\n\ts_nop 0\n\tglobal_load_lds_dwordx4 %0, off nt" :: "v"(g), "s"(L_IMG + i * 1024) : "memory");
      }
      asm volatile("s_mov_b32 m0, %0" :: "s"(keep));
    }
  }
  float hval = 0.f;
  if (w == 0) {
    u32x4 yv[4], ls;
    bool done = false;
    const unsigned yo = WS_YG + (unsigned)((rb * 128 + cb) * 512 + 2 * lane) * 8;
    const unsigned lo_ = WS_LG + (unsigned)((cb & (REP_L - 1)) * 256 + rb * 128 + 2 * lane) * 8;
    for (unsigned spins = 0; spins < SPIN_CAP; ++spins) {
      CFENCE();
      if (!done) {
#pragma unroll
        for (int s = 0; s < 4; ++s) yv[s] = LD16(yo + s * 1024);
        ls = LD16(lo_);
        bool ok = ls[1] == E && ls[3] == E;
#pragma unroll
        for (int s = 0; s < 4; ++s) ok &= yv[s][1] == E && yv[s][3] == E;
        done = ok;
      }
      const u64 pend = __ballot(!done);
      if (pend == 0) break;
      if (__builtin_popcountll(pend) > 16) __builtin_amdgcn_s_sleep(12); else __builtin_amdgcn_s_sleep(1);
    }
    STAMP(9);
    const float la = __uint_as_float(ls[0]), lb = __uint_as_float(ls[2]);
    float mx = fmaxf(la, lb); mx = fmaxf(mx, dpp<XOR1>(mx));
    const float ea = ex2(la - mx), eb = ex2(lb - mx);
    float den = ea + eb; den += dpp<XOR1>(den);
    const float inv = frcp(den);
    const float wa = ea * inv, wb = eb * inv, wa2 = dpp<XOR1>(wa), wb2 = dpp<XOR1>(wb);
    const bool cp = lane & 1;
    const float w0 = cp ? wa2 : wa, w1 = cp ? wb2 : wb, w2 = cp ? wa : wa2, w3 = cp ? wb : wb2;
    const float v0 = fmaf(w3, __uint_as_float(yv[3][0]), fmaf(w2, __uint_as_float(yv[2][0]), fmaf(w1, __uint_as_float(yv[1][0]), w0 * __uint_as_float(yv[0][0]))));
    const float v1 = fmaf(w3, __uint_as_float(yv[3][2]), fmaf(w2, __uint_as_float(yv[2][2]), fmaf(w1, __uint_as_float(yv[1][2]), w0 * __uint_as_float(yv[0][2]))));
    float val = x16_sum(rs32(v0, v1));
    val += dpp<ROR8>(val); val += dpp<ROR4>(val); val += dpp<XOR2>(val);
    hval = rbf(__uint_as_float((unsigned)xres_raw << 16) + rbf(val));
    auto hs = __builtin_amdgcn_permlane32_swap(__float_as_uint(hval), __float_as_uint(hval), false, false);
    const unsigned pair = (hs[0] >> 16) | (hs[1] & 0xffff0000u);
    const unsigned pair2 = dppu<XOR1>(pair);
    if (lane < 2 * REP_H && !cp) ST16((u32x4{pair, E, pair2, E}), WS_HG + (unsigned)((lane >> 1) * 512 + rb * 256 + 2 * cb) * 8);
  }
  STAMP(10);
  if (w == 0) {
    unsigned rv;
    for (unsigned spins = 0; spins < (SPIN_CAP << 4); ++spins) { rv = *role; if (rv != 0u) break; __builtin_amdgcn_s_sleep(1); }
    fj = __builtin_amdgcn_readfirstlane(rv) - 1;
  }
  const bool is_ffn = fj < NFFN;
  if (is_ffn) {
    u32x4 wdp[4], bfr[8];
    {
      const unsigned short* dp = p.wd + (size_t)(16 * fj + 2 * r) * D + 128 * w + 8 * c;
      const u32x4 w0 = ldnt(dp), w1 = ldnt(dp + D), w2 = ldnt(dp + 8 * D), w3 = ldnt(dp + 9 * D);
      asm volatile("s_waitcnt vmcnt(4)" ::: "memory");
      __syncthreads();
      const unsigned base = (unsigned)(L_IMG + (w & 1) * 16384 + (256 * (w >> 1) + 8 * r + ((lane & 15) >> 2)) * 32 + (lane & 3) * 8);
#pragma unroll
      for (int ks = 0; ks < 8; ++ks) {
        const s16x4 lo = __builtin_amdgcn_ds_read_tr16_b64_v4i16((LAS s16x4*)(size_t)(base + ks * 1024));
        const s16x4 hi = __builtin_amdgcn_ds_read_tr16_b64_v4i16((LAS s16x4*)(size_t)(base + ks * 1024 + 128));
        const u32x2 l2 = __builtin_bit_cast(u32x2, lo), h2 = __builtin_bit_cast(u32x2, hi);
        bfr[ks] = u32x4{l2[0], l2[1], h2[0], h2[1]};
      }
#pragma unroll
      for (int k = 0; k < 4; ++k) {
        wdp[0][k] = perm_lo(w0[k], w1[k]); wdp[1][k] = perm_hi(w0[k], w1[k]);
        wdp[2][k] = perm_lo(w2[k], w3[k]); wdp[3][k] = perm_hi(w2[k], w3[k]);
      }
    }
    u32x4 hg;
    {
      const unsigned ho = WS_HG + (unsigned)((bid & (REP_H - 1)) * 512 + 128 * w + 2 * lane) * 8;
      for (unsigned spins = 0; spins < SPIN_CAP; ++spins) {
        CFENCE();
        hg = LD16(ho);
        if (__all(hg[1] == E && hg[3] == E)) break;
      }
    }
    STAMP(11);
    {
      const float v0 = lo2f(hg[0]), v1 = hi2f(hg[0]), v2 = lo2f(hg[2]), v3 = hi2f(hg[2]);
      const float c0 = __uint_as_float(__builtin_amdgcn_readfirstlane(__float_as_uint(v0)));
      const float d0 = v0 - c0, d1 = v1 - c0, d2 = v2 - c0, d3 = v3 - c0;
      float s = (d0 + d1) + (d2 + d3), q = fmaf(d0, d0, fmaf(d1, d1, fmaf(d2, d2, d3 * d3)));
      float sq = x16_sum(rs32(s, q));
      sq = row_sum16(sq);
      auto t2 = __builtin_amdgcn_permlane32_swap(__float_as_uint(sq), __float_as_uint(sq), false, false);
      const float S = __uint_as_float(t2[0]), Q = __uint_as_float(t2[1]);
      if (lane == 0) *(float2*)(lds + L_ST + w * 8) = make_float2(c0 + S * (1.0f / 256), Q - S * S * (1.0f / 256));
      __syncthreads();
      const f32x4 stt = *(const f32x4*)(lds + L_ST + (w >> 1) * 16);
      const float dm = stt[0] - stt[2];
      float mb, rr;
      ln_consts(0.5f * (stt[0] + stt[2]), fmaxf((stt[1] + stt[3] + 128.0f * dm * dm) * (1.0f / D), 0.f), mb, rr);
      *(u32x2*)(lds + L_XN + (128 * w + 2 * lane) * 4) = u32x2{ln_pair(hg[0], mb, rr), ln_pair(hg[2], mb, rr)};
    }
    __syncthreads();
    {
      const unsigned char* xa = lds + L_XN + ((lane & 1) * 256 + 128 * (w >> 1) + 4 * r) * 4;
      f32x4 d = {0.f, 0.f, 0.f, 0.f};
#pragma unroll
      for (int ks = 0; ks < 8; ++ks) {
        const u32x4 a = *(const u32x4*)(xa + ks * 64);
        d = __builtin_amdgcn_mfma_f32_16x16x32_bf16(__builtin_bit_cast(bf16x8, a), __builtin_bit_cast(bf16x8, bfr[ks]), d, 0, 0, 0);
      }
      if (lane < 16) *(float2*)(lds + L_GP + (w * 16 + lane) * 8) = make_float2(d[0], d[1]);
    }
    __syncthreads();
    unsigned a00, a01, a10, a11;
    {
      const float* gp = (const float*)(lds + L_GP) + (lane & 15) * 2 + ((lane >> 4) & 1);
      const float g = rbf(gp[0] + gp[64]), u = rbf(gp[32] + gp[96]);
      const float inner = 0.7978845608028654f * (g + 0.044715f * g * g * g);
      const float ex = ex2(inner * (2.0f * LOG2E));
      const float th = 1.0f - 2.0f * frcp(ex + 1.0f);
      const float ge = rbf(0.5f * g * (1.0f + th));
      const unsigned ab = cvtpk(ge * u, 0.f) & 0xffffu;
      const unsigned pr = ab | (dppu<XOR1>(ab) << 16);
      a00 = __builtin_amdgcn_ds_bpermute((2 * r) * 4, (int)pr); a01 = __builtin_amdgcn_ds_bpermute((2 * r + 8) * 4, (int)pr);
      a10 = __builtin_amdgcn_ds_bpermute((16 + 2 * r) * 4, (int)pr); a11 = __builtin_amdgcn_ds_bpermute((24 + 2 * r) * 4, (int)pr);
    }
    STAMP(12);
    float z0[8], z1[8];
#pragma unroll
    for (int k = 0; k < 4; ++k) {
      z0[2 * k] = dot2(wdp[2][k], a01, dot2(wdp[0][k], a00, 0.f)); z0[2 * k + 1] = dot2(wdp[3][k], a01, dot2(wdp[1][k], a00, 0.f));
      z1[2 * k] = dot2(wdp[2][k], a11, dot2(wdp[0][k], a10, 0.f)); z1[2 * k + 1] = dot2(wdp[3][k], a11, dot2(wdp[1][k], a10, 0.f));
    }
    const float p00 = rs16(rs32(z0[0], z0[4]), rs32(z0[2], z0[6])), p01 = rs16(rs32(z0[1], z0[5]), rs32(z0[3], z0[7]));
    const float p10 = rs16(rs32(z1[0], z1[4]), rs32(z1[2], z1[6])), p11 = rs16(rs32(z1[1], z1[5]), rs32(z1[3], z1[7]));
    ST16((u32x4{__float_as_uint(p00), E, __float_as_uint(p01), E}), zoff + fj * 32);
    ST16((u32x4{__float_as_uint(p10), E, __float_as_uint(p11), E}), zoff + fj * 32 + 128 * 384 * 8);
    STAMP(13);
    if (w != 0) { DUMP(); return; }
  } else {
    __builtin_amdgcn_s_sleep(40);
  }
  {
    float z0 = 0.f, z1 = 0.f;
    const unsigned zo = WS_ZG + (unsigned)(bid * 384 + 2 * lane) * 8;
    const bool live2 = lane < 44;
    for (unsigned spins = 0; spins < SPIN_CAP; ++spins) {
      CFENCE();
      const u32x4 g0 = LD16(zo), g1 = LD16(zo + 1024), g2 = LD16(live2 ? zo + 2048 : zo);
      const bool ok = g0[1] == E && g0[3] == E && g1[1] == E && g1[3] == E && g2[1] == E && g2[3] == E;
      z0 = __uint_as_float(g0[0]) + __uint_as_float(g1[0]) + (live2 ? __uint_as_float(g2[0]) : 0.f);
      z1 = __uint_as_float(g0[2]) + __uint_as_float(g1[2]) + (live2 ? __uint_as_float(g2[2]) : 0.f);
      if (__all(ok)) break;
      __builtin_amdgcn_s_sleep(2);
    }
    STAMP(14);
    float zs = x16_sum(rs32(z0, z1));
    zs += dpp<ROR8>(zs); zs += dpp<ROR4>(zs); zs += dpp<XOR2>(zs);
    const unsigned ob = cvtpk(hval + rbf(zs), 0.f) & 0xffffu;
    auto os = __builtin_amdgcn_permlane32_swap(ob, ob, false, false);
    if (lane < 2) *(GAS unsigned*)(p.out + rb * D + 4 * cb + 2 * lane) = os[0] | (os[1] << 16);
    if (bid == 0 && lane == 0) __hip_atomic_store((gu32*)(p.ws + WS_W), wcnt + 1u, RLX_AGENT);
  }
  STAMP(15);
  DUMP();
}

extern "C" void kernel_launch(void* const* d_in, const int* in_sizes, int n_in, void* d_out, int out_size, void* d_ws, size_t ws_size,
                              hipStream_t stream) {
  Params p;
  p.x = (const unsigned short*)d_in[0]; p.kc = (const unsigned short*)d_in[1]; p.vc = (const unsigned short*)d_in[2];
  p.wq = (const unsigned short*)d_in[3]; p.wo = (const unsigned short*)d_in[4];
  p.wg = (const unsigned short*)d_in[6]; p.wu = (const unsigned short*)d_in[7]; p.wd = (const unsigned short*)d_in[8];
  p.out = (unsigned short*)d_out; p.ws = (unsigned char*)d_ws;
  (void)hipFuncSetAttribute((const void*)fused_kernel, hipFuncAttributeMaxDynamicSharedMemorySize, LDS_BYTES);
  hipLaunchKernelGGL(fused_kernel, dim3(NWG), dim3(NT), LDS_BYTES, stream, p);
}
```

```cpp
#include <hip/hip_runtime.h>
#include <stdint.h>

#define DBG 0
#define TAU 0
#define TOFS 0
#define STAG 10
#define STEPBAR 0

typedef unsigned u32x4 __attribute__((ext_vector_type(4)));
typedef unsigned u32x2 __attribute__((ext_vector_type(2)));
typedef float f32x4 __attribute__((ext_vector_type(4)));
typedef float f32x2 __attribute__((ext_vector_type(2)));
typedef __bf16 bf16x2 __attribute__((ext_vector_type(2)));
typedef __bf16 bf16x8 __attribute__((ext_vector_type(8)));
typedef short s16x4 __attribute__((ext_vector_type(4)));
typedef unsigned long long u64;
#define GAS __attribute__((address_space(1)))
#define LAS __attribute__((address_space(3)))
#define CAS __attribute__((address_space(4)))
typedef GAS u64 gu64;
typedef GAS unsigned gu32;
#define RLX_AGENT __ATOMIC_RELAXED, __HIP_MEMORY_SCOPE_AGENT

constexpr int D = 512, H = 32, SK = 8192, DH = 128, F = 1376;
constexpr int NWG = 256, NT = 320, NFFN = 86;
constexpr int REP_H = 8;
constexpr int NRED = 64;
constexpr unsigned WS_W = 0;
constexpr unsigned WS_TK = 1024;
constexpr unsigned WS_QG = 4096;
constexpr unsigned WS_HG = WS_QG + 32768;
constexpr unsigned WS_YG = 131072;
constexpr unsigned WS_ZG = WS_YG + (1u << 20);
constexpr unsigned WS_USED = WS_ZG + 64 * 88 * 128;
constexpr size_t WS_DBG = (size_t)256 << 20;
constexpr unsigned SPIN_CAP = 1u << 16;
constexpr int AUX_SC1 = 16;

struct Params {
  const unsigned short* x; const unsigned short* kc; const unsigned short* vc; const unsigned short* wq; const unsigned short* wo;
  const unsigned short* wg; const unsigned short* wu; const unsigned short* wd;
  unsigned short* out; unsigned char* ws;
};

__device__ __forceinline__ float dot2(unsigned a, unsigned b, float acc) {
  return __builtin_amdgcn_fdot2_f32_bf16(__builtin_bit_cast(bf16x2, a), __builtin_bit_cast(bf16x2, b), acc, false);
}
__device__ __forceinline__ unsigned cvtpk(float lo, float hi) {
  const f32x2 f = {lo, hi};
  return __builtin_bit_cast(unsigned, __builtin_convertvector(f, bf16x2));
}
__device__ __forceinline__ float lo2f(unsigned u) { return __uint_as_float(u << 16); }
__device__ __forceinline__ float hi2f(unsigned u) { return __uint_as_float(u & 0xffff0000u); }
__device__ __forceinline__ float rbf(float x) { return lo2f(cvtpk(x, x)); }
template <int CTRL> __device__ __forceinline__ float dpp(float x) {
  return __builtin_bit_cast(float, __builtin_amdgcn_mov_dpp(__builtin_bit_cast(int, x), CTRL, 0xf, 0xf, true));
}
template <int CTRL> __device__ __forceinline__ unsigned dppu(unsigned x) {
  return (unsigned)__builtin_amdgcn_mov_dpp((int)x, CTRL, 0xf, 0xf, true);
}
constexpr int ROR1 = 0x121, ROR2 = 0x122, ROR4 = 0x124, ROR8 = 0x128, XOR1 = 0xB1, XOR2 = 0x4E, XOR7 = 0x141;
__device__ __forceinline__ float x16_sum(float x) {
  auto s = __builtin_amdgcn_permlane16_swap(__float_as_uint(x), __float_as_uint(x), false, false);
  return __uint_as_float(s[0]) + __uint_as_float(s[1]);
}
__device__ __forceinline__ float x32_sum(float x) {
  auto s = __builtin_amdgcn_permlane32_swap(__float_as_uint(x), __float_as_uint(x), false, false);
  return __uint_as_float(s[0]) + __uint_as_float(s[1]);
}
__device__ __forceinline__ float x16_max(float x) {
  auto s = __builtin_amdgcn_permlane16_swap(__float_as_uint(x), __float_as_uint(x), false, false);
  return fmaxf(__uint_as_float(s[0]), __uint_as_float(s[1]));
}
__device__ __forceinline__ float x32_max(float x) {
  auto s = __builtin_amdgcn_permlane32_swap(__float_as_uint(x), __float_as_uint(x), false, false);
  return fmaxf(__uint_as_float(s[0]), __uint_as_float(s[1]));
}
__device__ __forceinline__ float rs32(float a, float b) {
  auto s = __builtin_amdgcn_permlane32_swap(__float_as_uint(a), __float_as_uint(b), false, false);
  return __uint_as_float(s[0]) + __uint_as_float(s[1]);
}
__device__ __forceinline__ float rs16(float a, float b) {
  auto s = __builtin_amdgcn_permlane16_swap(__float_as_uint(a), __float_as_uint(b), false, false);
  return __uint_as_float(s[0]) + __uint_as_float(s[1]);
}
__device__ __forceinline__ float row_sum16(float x) {
  x += dpp<ROR8>(x); x += dpp<ROR4>(x); x += dpp<ROR2>(x); x += dpp<ROR1>(x); return x;
}
__device__ __forceinline__ unsigned perm_lo(unsigned a, unsigned b) { return __builtin_amdgcn_perm(b, a, 0x05040100u); }
__device__ __forceinline__ unsigned perm_hi(unsigned a, unsigned b) { return __builtin_amdgcn_perm(b, a, 0x07060302u); }
__device__ __forceinline__ u32x4 ldnt(const void* p) { return __builtin_nontemporal_load((const GAS u32x4*)p); }
__device__ __forceinline__ u32x4 ldpl(const void* p) { return *(const GAS u32x4*)p; }
__device__ __forceinline__ unsigned mix32(unsigned h) {
  h ^= h >> 16; h *= 0x85ebca6bu; h ^= h >> 13; h *= 0xc2b2ae35u; h ^= h >> 16; return h;
}
__device__ __forceinline__ float frcp(float x) { return __builtin_amdgcn_rcpf(x); }
__device__ __forceinline__ float ex2(float x) { return __builtin_amdgcn_exp2f(x); }
#define SB() __builtin_amdgcn_sched_barrier(0)
#define CFENCE() asm volatile("" ::: "memory")
#define PIN2(a, b) asm volatile("" : "+v"(a), "+v"(b) :: "memory")
#define PIN4(a) asm volatile("" : "+v"(a[0]), "+v"(a[1]), "+v"(a[2]), "+v"(a[3]) :: "memory")
#define PIN8(a) asm volatile("" : "+v"(a[0]), "+v"(a[1]), "+v"(a[2]), "+v"(a[3]), "+v"(a[4]), "+v"(a[5]), "+v"(a[6]), "+v"(a[7]) :: "memory")
#define STAMP(i) do {} while (0)
#define DUMP() do {} while (0)
#define ST16(v, off) __builtin_amdgcn_raw_buffer_store_b128((v), rs, (int)(off), 0, AUX_SC1)
#define ST8(v, off) __builtin_amdgcn_raw_buffer_store_b64((v), rs, (int)(off), 0, AUX_SC1)
#define LD16(off) __builtin_amdgcn_raw_buffer_load_b128(rs, (int)(off), 0, AUX_SC1)
#define LD8(off) __builtin_amdgcn_raw_buffer_load_b64(rs, (int)(off), 0, AUX_SC1)

constexpr float LOG2E = 1.4426950408889634f;
constexpr float SCALE = 0.08838834764831845f;

__device__ __forceinline__ void quad_rs(const float (&y)[8], int lane, float& u0, float& u1) {
  const bool b1 = lane & 2, b0 = lane & 1;
  float t[4];
#pragma unroll
  for (int j = 0; j < 4; ++j) t[j] = (b1 ? y[j + 4] : y[j]) + dpp<XOR2>(b1 ? y[j] : y[j + 4]);
  u0 = (b0 ? t[2] : t[0]) + dpp<XOR1>(b0 ? t[0] : t[2]);
  u1 = (b0 ? t[3] : t[1]) + dpp<XOR1>(b0 ? t[1] : t[3]);
}
__device__ __forceinline__ void ln_consts(float mean, float var, float& mb, float& r) {
  mb = rbf(mean);
  r = frcp(rbf(__builtin_amdgcn_sqrtf(rbf(rbf(var) + 1e-5f))));
}
__device__ __forceinline__ unsigned ln_pair(unsigned raw, float mb, float r) {
  const unsigned n = cvtpk(lo2f(raw) - mb, hi2f(raw) - mb);
  return cvtpk(lo2f(n) * r, hi2f(n) * r);
}

constexpr int L_BIG = 0;
constexpr int L_IMG = 0;
constexpr int L_XN = 131072;
constexpr int L_Q = L_XN + 2048;
constexpr int L_OW = L_Q + 256;
constexpr int L_ML = L_OW + 2048;
constexpr int L_OP = L_ML + 64;
constexpr int L_ST = L_OP + 1024;
constexpr int L_GP = L_ST + 64;
constexpr int L_RED = L_GP + 512;
constexpr int L_ROLE = L_RED + 512;
constexpr int LDS_BYTES = L_ROLE + 64;

struct Tile { u32x4 k[4]; u32x4 v[4]; };

__global__ __launch_bounds__(NT, 2) void fused_kernel(Params p) {
  extern __shared__ __attribute__((aligned(16))) unsigned char lds[];
  const int tid = threadIdx.x, lane = tid & 63, wv = __builtin_amdgcn_readfirstlane(tid >> 6), w = wv - 1;
  const int bid = blockIdx.x;
  STAMP(0);
  const int xcd = bid & 7, jj = bid >> 3;
  const int h = xcd * 4 + (jj >> 3), b = (jj >> 2) & 1, sp = jj & 3;
  const int lid = (b * H + h) * 4 + sp;
  const int r = lane >> 4, c = lane & 15;
  const __amdgpu_buffer_rsrc_t rs = __builtin_amdgcn_make_buffer_rsrc((void*)p.ws, 0, (int)WS_USED, 0x00020000);
  const unsigned wcnt = *(const CAS unsigned*)(size_t)(p.ws + WS_W);
  if (wv == 0) {
    __builtin_amdgcn_s_setprio(3);
    const u32x4 xraw = ldpl(p.x + b * D + lane * 8);
    u32x4 wq[32];
    const unsigned char* q_ = (const unsigned char*)p.wq + ((size_t)(32 * (lane >> 2)) * (H * DH) + h * DH + 32 * sp + 8 * (lane & 3)) * 2;
#pragma unroll
    for (int i = 0; i < 32; ++i) wq[i] = ldnt(q_ + (size_t)i * (H * DH * 2));
    CFENCE(); SB();
    __builtin_amdgcn_s_barrier();
    SB();
    const unsigned E = mix32(wcnt + 1u);
    if (lane == 0) {
      *(volatile LAS unsigned*)(size_t)(unsigned)L_ROLE = 0u;
      if (bid == 0) ST8((u32x2{0u, E}), WS_TK);
    }
    float xv[8];
#pragma unroll
    for (int i = 0; i < 4; ++i) { xv[2 * i] = lo2f(xraw[i]); xv[2 * i + 1] = hi2f(xraw[i]); }
    const float c0 = __uint_as_float(__builtin_amdgcn_readfirstlane(__float_as_uint(xv[0])));
    float s = 0.f, q = 0.f;
#pragma unroll
    for (int i = 0; i < 8; ++i) { const float d = xv[i] - c0; s += d; q = fmaf(d, d, q); }
    s = row_sum16(s); q = row_sum16(q);
    s = x32_sum(x16_sum(s)); q = x32_sum(x16_sum(q));
    const float ms = s * (1.0f / D);
    float mb, rr;
    ln_consts(c0 + ms, fmaxf(q * (1.0f / D) - ms * ms, 0.f), mb, rr);
    u32x4* xs = (u32x4*)(lds + L_XN);
    xs[lane] = u32x4{ln_pair(xraw[0], mb, rr), ln_pair(xraw[1], mb, rr), ln_pair(xraw[2], mb, rr), ln_pair(xraw[3], mb, rr)};
    SB();
#pragma unroll
    for (int i = 0; i < 32; i += 8)
      asm volatile("" : "+v"(wq[i]), "+v"(wq[i + 1]), "+v"(wq[i + 2]), "+v"(wq[i + 3]), "+v"(wq[i + 4]), "+v"(wq[i + 5]), "+v"(wq[i + 6]), "+v"(wq[i + 7]));
    STAMP(1);
    u32x4 xp[4];
#pragma unroll
    for (int i = 0; i < 4; ++i) xp[i] = xs[(lane & ~3) + i];
    float acc[8] = {0, 0, 0, 0, 0, 0, 0, 0};
#pragma unroll
    for (int pp = 0; pp < 16; ++pp)
#pragma unroll
      for (int k = 0; k < 4; ++k) {
        acc[2 * k] = dot2(perm_lo(wq[2 * pp][k], wq[2 * pp + 1][k]), xp[pp >> 2][pp & 3], acc[2 * k]);
        acc[2 * k + 1] = dot2(perm_hi(wq[2 * pp][k], wq[2 * pp + 1][k]), xp[pp >> 2][pp & 3], acc[2 * k + 1]);
      }
    const float t0 = rs32(acc[0], acc[4]), t1 = rs32(acc[1], acc[5]), t2 = rs32(acc[2], acc[6]), t3 = rs32(acc[3], acc[7]);
    float u0 = rs16(t0, t2), u1 = rs16(t1, t3);
    u0 += dpp<ROR8>(u0); u1 += dpp<ROR8>(u1); u0 += dpp<ROR4>(u0); u1 += dpp<ROR4>(u1);
    const unsigned qg = WS_QG + (unsigned)((b * H + h) * 64) * 8;
    {
      const unsigned qp = (unsigned)__builtin_amdgcn_ds_bpermute((((lane >> 2) & 3) + 16 * (lane & 1) + 32 * ((lane >> 1) & 1)) * 4, (int)cvtpk(u0, u1));
      if (lane < 16) ST8((u32x2{qp, E}), qg + (16 * sp + lane) * 8);
    }
    STAMP(2);
    u32x2 g;
    for (unsigned spins = 0; spins < SPIN_CAP; ++spins) {
      CFENCE();
      g = LD8(qg + lane * 8);
      if (__all(g[1] == E)) break;
    }
    ((unsigned*)(lds + L_Q))[lane] = g[0];
    __syncthreads();
    STAMP(3);
    return;
  }
#define ROTM 0
  const int rot = ROTM == 0 ? (lid & 7) * 4 : ROTM == 1 ? 0 : ROTM == 2 ? ((lid * 5) & 31) : ROTM == 3 ? ((lid >> 2) & 7) * 4 : ((lid * 13 + (lid >> 3)) & 31);
  const unsigned char* kbase = (const unsigned char*)p.kc + ((size_t)lid * 2048 * DH + (size_t)w * 16 * DH) * 2 + lane * 16;
  const unsigned char* vbase = (const unsigned char*)p.vc + ((size_t)lid * 2048 * DH + (size_t)w * 16 * DH) * 2 + lane * 16;
#define TOFF(i) ((size_t)((((i) + rot) & 31)) * (4 * 16 * DH * 2))
#define LOADK(T, i) do { const unsigned char* q_ = kbase + TOFF(i); _Pragma("unroll") for (int t_ = 0; t_ < 4; ++t_) T.k[t_] = ldnt(q_ + t_ * 1024); CFENCE(); SB(); } while (0)
#define LOADV(T, i) do { const unsigned char* q_ = vbase + TOFF(i); _Pragma("unroll") for (int t_ = 0; t_ < 4; ++t_) T.v[t_] = ldnt(q_ + t_ * 1024); CFENCE(); SB(); } while (0)
  Tile T0, T1, T2, T3;
  SB();
  __builtin_amdgcn_s_barrier();
  SB();
#define PACE() asm volatile("s_waitcnt vmcnt(12)" ::: "memory")
  LOADK(T0, 0); LOADV(T0, 0); LOADK(T1, 1); LOADV(T1, 1); PACE(); LOADK(T2, 2); PACE(); LOADV(T2, 2); PACE(); LOADK(T3, 3); PACE(); LOADV(T3, 3);
  {
    unsigned keep;
    asm volatile("s_mov_b32 %0, m0" : "=s"(keep));
#pragma unroll
    for (int j = 0; j < 4; ++j) {
      const unsigned char* kp = kbase + TOFF(4 + j);
      const unsigned char* vp = vbase + TOFF(4 + j);
      PACE();
#pragma unroll
      for (int t = 0; t < 4; ++t)
        asm volatile("s_mov_b32 m0, %1\n\ts_nop 0\n\tglobal_load_lds_dwordx4 %0, off nt" :: "v"(kp + t * 1024), "s"(L_BIG + w * 32768 + j * 8192 + t * 1024) : "memory");
      PACE();
#pragma unroll
      for (int t = 0; t < 4; ++t)
        asm volatile("s_mov_b32 m0, %1\n\ts_nop 0\n\tglobal_load_lds_dwordx4 %0, off nt" :: "v"(vp + t * 1024), "s"(L_BIG + w * 32768 + j * 8192 + 4096 + t * 1024) : "memory");
    }
    asm volatile("s_mov_b32 m0, %0" :: "s"(keep));
  }
  const unsigned E = mix32(wcnt + 1u);
  const unsigned T16 = (((wcnt + 1u) * 40503u) & 0xffffu) << 16;
  __syncthreads();
  const u32x4 q4 = *(const u32x4*)(lds + L_Q + c * 16);
  STAMP(3);
  float m = -INFINITY, l = 0.f;
  float acc[8] = {0, 0, 0, 0, 0, 0, 0, 0};
  auto qk = [&](const Tile& T, float (&s)[4]) {
#pragma unroll
    for (int t = 0; t < 4; ++t) {
      float d = dot2(T.k[t][0], q4[0], 0.f);
      d = dot2(T.k[t][1], q4[1], d); d = dot2(T.k[t][2], q4[2], d); d = dot2(T.k[t][3], q4[3], d);
      s[t] = d;
    }
  };
  auto sm = [&](float (&s)[4]) {
#pragma unroll
    for (int t = 0; t < 4; ++t) s[t] = row_sum16(s[t]);
    const unsigned a01 = cvtpk(s[0], s[1]), a23 = cvtpk(s[2], s[3]);
    const unsigned b01 = cvtpk(lo2f(a01) * SCALE, hi2f(a01) * SCALE), b23 = cvtpk(lo2f(a23) * SCALE, hi2f(a23) * SCALE);
    const float z0 = lo2f(b01) * LOG2E, z1 = hi2f(b01) * LOG2E, z2 = lo2f(b23) * LOG2E, z3 = hi2f(b23) * LOG2E;
    const float mn = fmaxf(fmaxf(fmaxf(z0, z1), fmaxf(z2, z3)), m);
    const float al = ex2(m - mn);
    s[0] = ex2(z0 - mn); s[1] = ex2(z1 - mn); s[2] = ex2(z2 - mn); s[3] = ex2(z3 - mn);
    l = fmaf(l, al, (s[0] + s[1]) + (s[2] + s[3]));
    m = mn;
#pragma unroll
    for (int k = 0; k < 8; ++k) acc[k] *= al;
  };
  auto pv = [&](const Tile& T, const float (&s)[4]) {
#pragma unroll
    for (int t = 0; t < 4; ++t)
#pragma unroll
      for (int k = 0; k < 4; ++k) {
        acc[2 * k] = fmaf(s[t], lo2f(T.v[t][k]), acc[2 * k]);
        acc[2 * k + 1] = fmaf(s[t], hi2f(T.v[t][k]), acc[2 * k + 1]);
      }
  };
#define SBAR() do {} while (0)
#define WAITT(n) do {} while (0)
#define STEP(T, i_next) do { float s_[4]; qk(T, s_); PIN4(s_); SB(); WAITT(i_next); SB(); LOADK(T, i_next); sm(s_); pv(T, s_); PIN8(acc); SB(); LOADV(T, i_next); SBAR(); } while (0)
#define STEP_LAST(T) do { float s_[4]; qk(T, s_); sm(s_); pv(T, s_); PIN8(acc); SB(); } while (0)
  STEP(T0, 8); STEP(T1, 9); STEP(T2, 10); STEP(T3, 11);
  {
    const u32x4* tl = (const u32x4*)(lds + L_BIG + w * 32768) + lane;
#define STEP_LDS(j, cnt) do { asm volatile("s_waitcnt vmcnt(" #cnt ")" ::: "memory"); Tile TL; \
      _Pragma("unroll") for (int t_ = 0; t_ < 4; ++t_) { TL.k[t_] = tl[(j * 8 + t_) * 64]; TL.v[t_] = tl[(j * 8 + 4 + t_) * 64]; } \
      float s_[4]; qk(TL, s_); sm(s_); pv(TL, s_); PIN8(acc); SB(); SBAR(); } while (0)
    STEP_LDS(0, 56); STEP_LDS(1, 48); STEP_LDS(2, 40); STEP_LDS(3, 32);
  }
  {
    const unsigned char* wob = (const unsigned char*)p.wo + ((size_t)(h * DH + 32 * (lane & 3)) * D + 128 * w + 8 * (lane >> 2)) * 2;
    u32x4* wl = (u32x4*)(lds + L_BIG + w * 32768) + lane;
    for (int it = 0; it < 5; ++it) {
      u32x4 wo[8];
      const int ch = it < 4 ? it : 3;
#pragma unroll
      for (int i = 0; i < 8; ++i) wo[i] = ldnt(wob + (size_t)(8 * ch + i) * (D * 2));
      CFENCE(); SB();
      STEP(T0, 4 * it + 12); STEP(T1, 4 * it + 13); STEP(T2, 4 * it + 14); STEP(T3, 4 * it + 15);
#pragma unroll
      for (int pp = 0; pp < 4; ++pp) {
        u32x4 lo, hi;
#pragma unroll
        for (int k = 0; k < 4; ++k) { lo[k] = perm_lo(wo[2 * pp][k], wo[2 * pp + 1][k]); hi[k] = perm_hi(wo[2 * pp][k], wo[2 * pp + 1][k]); }
        wl[(8 * ch + 2 * pp) * 64] = lo; wl[(8 * ch + 2 * pp + 1) * 64] = hi;
      }
      SB();
    }
  }
  const bool is_red = bid < NRED;
  const int rb = (bid >> 5) & 1, blk = bid & 31;
  unsigned yoff = WS_YG + (unsigned)((((b * 32 + 8 * w + (lane >> 3)) * 4 + sp) * 32 + h) * 128 + (lane & 7) * 16);
  unsigned zoff = WS_ZG + (unsigned)((8 * w + (lane >> 3)) * 88 * 128 + (lane & 7) * 16);
  PIN2(yoff, zoff);
  const unsigned xres2 = *(const GAS unsigned*)(p.x + rb * D + 16 * blk + 2 * (lane & 7));
  u32x4 wop[32];
  const u32x4* wlr = (const u32x4*)(lds + L_BIG + w * 32768) + lane;
  STEP_LAST(T0);
#pragma unroll
  for (int i = 0; i < 8; ++i) wop[i] = wlr[i * 64];
  SB();
  STEP_LAST(T1);
#pragma unroll
  for (int i = 8; i < 16; ++i) wop[i] = wlr[i * 64];
  SB();
  STEP_LAST(T2);
#pragma unroll
  for (int i = 16; i < 24; ++i) wop[i] = wlr[i * 64];
  SB();
  STEP_LAST(T3);
#pragma unroll
  for (int i = 24; i < 32; ++i) wop[i] = wlr[i * 64];
  STAMP(4);
  {
    const float mw = x32_max(x16_max(m));
    const float e = ex2(m - mw);
    const float lw = x32_sum(x16_sum(l * e));
    float a[8];
#pragma unroll
    for (int k = 0; k < 8; ++k) a[k] = acc[k] * e;
    const float t0 = rs32(a[0], a[4]), t1 = rs32(a[1], a[5]), t2 = rs32(a[2], a[6]), t3 = rs32(a[3], a[7]);
    const float u0 = rs16(t0, t2), u1 = rs16(t1, t3);
    float* ow = (float*)(lds + L_OW) + w * 128 + c * 8 + (r >> 1) * 4 + (r & 1) * 2;
    *(float2*)ow = make_float2(u0, u1);
    if (lane == 0) { ((float*)(lds + L_ML))[w] = mw; ((float*)(lds + L_ML))[4 + w] = lw; }
  }
  STAMP(5);
  __syncthreads();
  STAMP(6);
  {
    const f32x4 m4 = *(const f32x4*)(lds + L_ML), l4 = *(const f32x4*)(lds + L_ML + 16);
    const float M = fmaxf(fmaxf(m4[0], m4[1]), fmaxf(m4[2], m4[3]));
    float L = 0.f, o0 = 0.f, o1 = 0.f;
#pragma unroll
    for (int ww = 0; ww < 4; ++ww) {
      const float e = ex2(m4[ww] - M);
      L = fmaf(e, l4[ww], L);
      const float2 v = *(const float2*)((const float*)(lds + L_OW) + ww * 128 + 2 * lane);
      o0 = fmaf(e, v.x, o0); o1 = fmaf(e, v.y, o1);
    }
    const float inv = frcp(L);
    unsigned* op = (unsigned*)(lds + L_OP) + w * 64;
    op[lane] = cvtpk(o0 * inv, o1 * inv);
    const unsigned lse = __float_as_uint(M + __builtin_amdgcn_logf(L));
    u32x4 ov[4];
#pragma unroll
    for (int i = 0; i < 4; ++i) ov[i] = *(const u32x4*)(op + 16 * (lane & 3) + 4 * i);
    float y[8] = {0, 0, 0, 0, 0, 0, 0, 0};
#pragma unroll
    for (int pp = 0; pp < 16; ++pp) {
      const unsigned o2 = ov[pp >> 2][pp & 3];
#pragma unroll
      for (int k = 0; k < 4; ++k) {
        y[2 * k] = dot2(wop[2 * pp][k], o2, y[2 * k]);
        y[2 * k + 1] = dot2(wop[2 * pp + 1][k], o2, y[2 * k + 1]);
      }
    }
    float u0, u1;
    quad_rs(y, lane, u0, u1);
    ST16((u32x4{__float_as_uint(u0), T16 | (lse >> 16), __float_as_uint(u1), T16 | (lse & 0xffffu)}), yoff);
  }
  STAMP(7);
  if (is_red) {
    u32x4 yv[4];
    const unsigned yo = WS_YG + (unsigned)((((rb * 32 + blk) * 4) * 32 + 8 * w) * 128 + lane * 16);
#define H1_LOAD(Y) do { _Pragma("unroll") for (int s_ = 0; s_ < 4; ++s_) Y[s_] = LD16(yo + s_ * 4096); } while (0)
#define H1_BAD(Y) (((Y[0][1] ^ T16) | (Y[0][3] ^ T16) | (Y[1][1] ^ T16) | (Y[1][3] ^ T16) | (Y[2][1] ^ T16) | (Y[2][3] ^ T16) | (Y[3][1] ^ T16) | (Y[3][3] ^ T16)) >> 16)
    for (unsigned spins = 0; spins < SPIN_CAP; ++spins) {
      CFENCE();
      H1_LOAD(yv);
      const u64 pend = __ballot(H1_BAD(yv) != 0u);
      if (pend == 0) break;
      if (__builtin_popcountll(pend) > 16) __builtin_amdgcn_s_sleep(8);
    }
    STAMP(9);
    {
      float ls[4];
#pragma unroll
      for (int s_ = 0; s_ < 4; ++s_) ls[s_] = __uint_as_float((yv[s_][1] << 16) | (yv[s_][3] & 0xffffu));
      const float mx = fmaxf(fmaxf(ls[0], ls[1]), fmaxf(ls[2], ls[3]));
      const float e0 = ex2(ls[0] - mx), e1 = ex2(ls[1] - mx), e2 = ex2(ls[2] - mx), e3 = ex2(ls[3] - mx);
      const float inv = frcp((e0 + e1) + (e2 + e3));
      const float v0 = inv * fmaf(e3, __uint_as_float(yv[3][0]), fmaf(e2, __uint_as_float(yv[2][0]), fmaf(e1, __uint_as_float(yv[1][0]), e0 * __uint_as_float(yv[0][0]))));
      const float v1 = inv * fmaf(e3, __uint_as_float(yv[3][2]), fmaf(e2, __uint_as_float(yv[2][2]), fmaf(e1, __uint_as_float(yv[1][2]), e0 * __uint_as_float(yv[0][2]))));
      float val = x16_sum(rs32(v0, v1));
      val += dpp<ROR8>(val);
      if ((lane & 0x18) == 0) ((float*)(lds + L_RED))[w * 16 + 2 * (lane & 7) + (lane >> 5)] = val;
    }
    __syncthreads();
    float h0 = 0.f, h1 = 0.f;
    if (w == 0) {
      const float2* rp = (const float2*)(lds + L_RED) + (lane & 7);
      const float2 a0 = rp[0], a1 = rp[8], a2 = rp[16], a3 = rp[24];
      h0 = rbf(lo2f(xres2) + rbf((a0.x + a1.x) + (a2.x + a3.x)));
      h1 = rbf(hi2f(xres2) + rbf((a0.y + a1.y) + (a2.y + a3.y)));
      ST16((u32x4{__float_as_uint(h0), E, __float_as_uint(h1), E}), WS_HG + (unsigned)((((lane >> 3) * 2 + rb) * 512 + 16 * blk + 2 * (lane & 7)) * 8));
    }
    STAMP(10);
    __builtin_amdgcn_s_sleep(24);
    {
      const unsigned zo = WS_ZG + (unsigned)(((rb * 32 + blk) * 88 + 8 * w) * 128 + lane * 16);
      const bool live2 = w < 2 || (w == 2 && lane < 48);
      const unsigned zo2 = live2 ? zo + 8192 : zo;
      u32x4 g0, g1, g2;
      for (unsigned spins = 0; spins < SPIN_CAP; ++spins) {
        CFENCE();
        g0 = LD16(zo); g1 = LD16(zo + 4096); g2 = LD16(zo2);
        if (__all(g0[1] == E && g0[3] == E && g1[1] == E && g1[3] == E && g2[1] == E && g2[3] == E)) break;
      }
      STAMP(14);
      const float z0 = __uint_as_float(g0[0]) + __uint_as_float(g1[0]) + (live2 ? __uint_as_float(g2[0]) : 0.f);
      const float z1 = __uint_as_float(g0[2]) + __uint_as_float(g1[2]) + (live2 ? __uint_as_float(g2[2]) : 0.f);
      float zs = x16_sum(rs32(z0, z1));
      zs += dpp<ROR8>(zs);
      if ((lane & 0x18) == 0) ((float*)(lds + L_RED))[64 + w * 16 + 2 * (lane & 7) + (lane >> 5)] = zs;
    }
    __syncthreads();
    if (w == 0) {
      const float2* rp = (const float2*)(lds + L_RED + 256) + (lane & 7);
      const float2 a0 = rp[0], a1 = rp[8], a2 = rp[16], a3 = rp[24];
      const unsigned o2 = cvtpk(h0 + rbf((a0.x + a1.x) + (a2.x + a3.x)), h1 + rbf((a0.y + a1.y) + (a2.y + a3.y)));
      if (lane < 8) *(GAS unsigned*)(p.out + rb * D + 16 * blk + 2 * lane) = o2;
      if (bid == 0 && lane == 0) __hip_atomic_store((gu32*)(p.ws + WS_W), wcnt + 1u, RLX_AGENT);
    }
    STAMP(15);
    DUMP();
    return;
  }
  volatile LAS unsigned* role = (volatile LAS unsigned*)(size_t)(unsigned)L_ROLE;
  if (w == 0 && lane == 0) {
    unsigned tk = NFFN;
    for (unsigned spins = 0; spins < SPIN_CAP; ++spins) {
      const u64 old = __hip_atomic_fetch_add((gu64*)(p.ws + WS_TK), 1ull, RLX_AGENT);
      tk = (unsigned)old;
      if ((unsigned)(old >> 32) == E) break;
      tk = NFFN;
      for (; spins < SPIN_CAP && (unsigned)(__hip_atomic_load((gu64*)(p.ws + WS_TK), RLX_AGENT) >> 32) != E; ++spins) __builtin_amdgcn_s_sleep(2);
    }
    *role = tk;
  }
  __syncthreads();
  const int fj = __builtin_amdgcn_readfirstlane(*role);
  if (fj >= NFFN) { DUMP(); return; }
  {
    u32x4 wdp[4], bfr[8];
    {
      const unsigned short* src = ((w & 2) ? p.wu : p.wg) + (size_t)(256 * (w & 1) + (lane >> 1)) * F + 16 * fj + 8 * (lane & 1);
      unsigned keep;
      asm volatile("s_mov_b32 %0, m0" : "=s"(keep));
#pragma unroll
      for (int i = 0; i < 8; ++i)
        asm volatile("s_mov_b32 m0, %1\n\ts_nop 0\n\tglobal_load_lds_dwordx4 %0, off nt" :: "v"(src + (size_t)i * 32 * F), "s"(L_IMG + (8 * w + i) * 1024) : "memory");
      asm volatile("s_mov_b32 m0, %0" :: "s"(keep));
      const unsigned short* dp = p.wd + (size_t)(16 * fj + 2 * (lane & 3)) * D + 128 * w + 8 * (lane >> 2);
      const u32x4 w0 = ldnt(dp), w1 = ldnt(dp + D), w2 = ldnt(dp + 8 * D), w3 = ldnt(dp + 9 * D);
      asm volatile("s_waitcnt vmcnt(4)" ::: "memory");
      __syncthreads();
      const unsigned base = (unsigned)(L_IMG + (w & 1) * 16384 + (256 * (w >> 1) + 8 * r + ((lane & 15) >> 2)) * 32 + (lane & 3) * 8);
#pragma unroll
      for (int ks = 0; ks < 8; ++ks) {
        const s16x4 lo = __builtin_amdgcn_ds_read_tr16_b64_v4i16((LAS s16x4*)(size_t)(base + ks * 1024));
        const s16x4 hi = __builtin_amdgcn_ds_read_tr16_b64_v4i16((LAS s16x4*)(size_t)(base + ks * 1024 + 128));
        const u32x2 l2 = __builtin_bit_cast(u32x2, lo), h2 = __builtin_bit_cast(u32x2, hi);
        bfr[ks] = u32x4{l2[0], l2[1], h2[0], h2[1]};
      }
#pragma unroll
      for (int k = 0; k < 4; ++k) {
        wdp[0][k] = perm_lo(w0[k], w1[k]); wdp[1][k] = perm_hi(w0[k], w1[k]);
        wdp[2][k] = perm_lo(w2[k], w3[k]); wdp[3][k] = perm_hi(w2[k], w3[k]);
      }
    }
    zoff += (unsigned)fj * 128;
    STAMP(10);
    u32x4 ha, hb;
    {
      const unsigned ho = WS_HG + (unsigned)((((bid & (REP_H - 1)) * 2 + (w >> 1)) * 512 + 256 * (w & 1) + 2 * lane) * 8);
      for (unsigned spins = 0; spins < SPIN_CAP; ++spins) {
        CFENCE();
        ha = LD16(ho); hb = LD16(ho + 1024);
        const u64 pend = __ballot(!(ha[1] == E && ha[3] == E && hb[1] == E && hb[3] == E));
        if (pend == 0) break;
        if (pend == ~0ull) __builtin_amdgcn_s_sleep(6);
      }
    }
    STAMP(11);
    {
      const float v0 = __uint_as_float(ha[0]), v1 = __uint_as_float(ha[2]), v2 = __uint_as_float(hb[0]), v3 = __uint_as_float(hb[2]);
      const float c0 = __uint_as_float(__builtin_amdgcn_readfirstlane(ha[0]));
      const float d0 = v0 - c0, d1 = v1 - c0, d2 = v2 - c0, d3 = v3 - c0;
      float s = (d0 + d1) + (d2 + d3), q = fmaf(d0, d0, fmaf(d1, d1, fmaf(d2, d2, d3 * d3)));
      float sq = x16_sum(rs32(s, q));
      sq = row_sum16(sq);
      auto t2 = __builtin_amdgcn_permlane32_swap(__float_as_uint(sq), __float_as_uint(sq), false, false);
      const float S = __uint_as_float(t2[0]), Q = __uint_as_float(t2[1]);
      if (lane == 0) *(float2*)(lds + L_ST + w * 8) = make_float2(c0 + S * (1.0f / 256), Q - S * S * (1.0f / 256));
      __syncthreads();
      const f32x4 stt = *(const f32x4*)(lds + L_ST + (w >> 1) * 16);
      const float dm = stt[0] - stt[2];
      float mb, rr;
      ln_consts(0.5f * (stt[0] + stt[2]), fmaxf((stt[1] + stt[3] + 128.0f * dm * dm) * (1.0f / D), 0.f), mb, rr);
      const unsigned n0 = cvtpk(v0 - mb, v1 - mb), n1 = cvtpk(v2 - mb, v3 - mb);
      unsigned* xn = (unsigned*)(lds + L_XN) + 128 * w + lane;
      xn[0] = cvtpk(lo2f(n0) * rr, hi2f(n0) * rr); xn[64] = cvtpk(lo2f(n1) * rr, hi2f(n1) * rr);
    }
    __syncthreads();
    {
      const unsigned char* xa = lds + L_XN + ((lane & 1) * 256 + 128 * (w >> 1) + 4 * r) * 4;
      f32x4 d = {0.f, 0.f, 0.f, 0.f};
#pragma unroll
      for (int ks = 0; ks < 8; ++ks) {
        const u32x4 a = *(const u32x4*)(xa + ks * 64);
        d = __builtin_amdgcn_mfma_f32_16x16x32_bf16(__builtin_bit_cast(bf16x8, a), __builtin_bit_cast(bf16x8, bfr[ks]), d, 0, 0, 0);
      }
      if (lane < 16) *(float2*)(lds + L_GP + (w * 16 + lane) * 8) = make_float2(d[0], d[1]);
    }
    __syncthreads();
    unsigned a00, a01, a10, a11;
    {
      const float* gp = (const float*)(lds + L_GP) + (lane & 15) * 2 + ((lane >> 4) & 1);
      const float g = rbf(gp[0] + gp[64]), u = rbf(gp[32] + gp[96]);
      const float inner = 0.7978845608028654f * (g + 0.044715f * g * g * g);
      const float ex = ex2(inner * (2.0f * LOG2E));
      const float th = 1.0f - 2.0f * frcp(ex + 1.0f);
      const float ge = rbf(0.5f * g * (1.0f + th));
      const unsigned ab = cvtpk(ge * u, 0.f) & 0xffffu;
      const unsigned pr = ab | (dppu<XOR1>(ab) << 16);
      const int rg = lane & 3;
      a00 = __builtin_amdgcn_ds_bpermute((2 * rg) * 4, (int)pr); a01 = __builtin_amdgcn_ds_bpermute((2 * rg + 8) * 4, (int)pr);
      a10 = __builtin_amdgcn_ds_bpermute((16 + 2 * rg) * 4, (int)pr); a11 = __builtin_amdgcn_ds_bpermute((24 + 2 * rg) * 4, (int)pr);
    }
    STAMP(12);
    float z0[8], z1[8];
#pragma unroll
    for (int k = 0; k < 4; ++k) {
      z0[2 * k] = dot2(wdp[2][k], a01, dot2(wdp[0][k], a00, 0.f)); z0[2 * k + 1] = dot2(wdp[3][k], a01, dot2(wdp[1][k], a00, 0.f));
      z1[2 * k] = dot2(wdp[2][k], a11, dot2(wdp[0][k], a10, 0.f)); z1[2 * k + 1] = dot2(wdp[3][k], a11, dot2(wdp[1][k], a10, 0.f));
    }
    float p00, p01, p10, p11;
    quad_rs(z0, lane, p00, p01); quad_rs(z1, lane, p10, p11);
    ST16((u32x4{__float_as_uint(p00), E, __float_as_uint(p01), E}), zoff);
    ST16((u32x4{__float_as_uint(p10), E, __float_as_uint(p11), E}), zoff + 32 * 88 * 128);
    STAMP(13);
  }
  DUMP();
}

extern "C" void kernel_launch(void* const* d_in, const int* in_sizes, int n_in, void* d_out, int out_size, void* d_ws, size_t ws_size,
                              hipStream_t stream) {
  Params p;
  p.x = (const unsigned short*)d_in[0]; p.kc = (const unsigned short*)d_in[1]; p.vc = (const unsigned short*)d_in[2];
  p.wq = (const unsigned short*)d_in[3]; p.wo = (const unsigned short*)d_in[4];
  p.wg = (const unsigned short*)d_in[6]; p.wu = (const unsigned short*)d_in[7]; p.wd = (const unsigned short*)d_in[8];
  p.out = (unsigned short*)d_out; p.ws = (unsigned char*)d_ws;
  (void)hipFuncSetAttribute((const void*)fused_kernel, hipFuncAttributeMaxDynamicSharedMemorySize, LDS_BYTES);
  hipLaunchKernelGGL(fused_kernel, dim3(NWG), dim3(NT), LDS_BYTES, stream, p);
}
```

```cpp
#include <hip/hip_runtime.h>
#include <stdint.h>

typedef unsigned u32x4 __attribute__((ext_vector_type(4)));
typedef unsigned u32x2 __attribute__((ext_vector_type(2)));
typedef float f32x4 __attribute__((ext_vector_type(4)));
typedef float f32x2 __attribute__((ext_vector_type(2)));
typedef __bf16 bf16x2 __attribute__((ext_vector_type(2)));
typedef __bf16 bf16x8 __attribute__((ext_vector_type(8)));
typedef short s16x4 __attribute__((ext_vector_type(4)));
typedef unsigned long long u64;
#define GAS __attribute__((address_space(1)))
#define LAS __attribute__((address_space(3)))
#define CAS __attribute__((address_space(4)))
typedef GAS u64 gu64;
typedef GAS unsigned gu32;
#define RLX_AGENT __ATOMIC_RELAXED, __HIP_MEMORY_SCOPE_AGENT

constexpr int D = 512, H = 32, SK = 8192, DH = 128, F = 1376;
constexpr int NWG = 256, NT = 320, NFFN = 86;
constexpr int REP_H = 8;
constexpr int NRED = 64;
constexpr unsigned WS_W = 0;
constexpr unsigned WS_TK = 1024;
constexpr unsigned WS_HG = 4096 + 32768;
constexpr unsigned WS_YG = 131072;
constexpr unsigned WS_ZG = WS_YG + (1u << 20);
constexpr unsigned WS_QG = WS_ZG + 64 * 88 * 128;
constexpr unsigned WS_USED = WS_QG + 262144;
constexpr unsigned SPIN_CAP = 1u << 16;
constexpr int AUX_SC1 = 16;

struct Params {
  const unsigned short* x; const unsigned short* kc; const unsigned short* vc; const unsigned short* wq; const unsigned short* wo;
  const unsigned short* wg; const unsigned short* wu; const unsigned short* wd;
  unsigned short* out; unsigned char* ws;
};

__device__ __forceinline__ float dot2(unsigned a, unsigned b, float acc) {
  return __builtin_amdgcn_fdot2_f32_bf16(__builtin_bit_cast(bf16x2, a), __builtin_bit_cast(bf16x2, b), acc, false);
}
__device__ __forceinline__ unsigned cvtpk(float lo, float hi) {
  const f32x2 f = {lo, hi};
  return __builtin_bit_cast(unsigned, __builtin_convertvector(f, bf16x2));
}
__device__ __forceinline__ float lo2f(unsigned u) { return __uint_as_float(u << 16); }
__device__ __forceinline__ float hi2f(unsigned u) { return __uint_as_float(u & 0xffff0000u); }
__device__ __forceinline__ float rbf(float x) { return lo2f(cvtpk(x, x)); }
template <int CTRL> __device__ __forceinline__ float dpp(float x) {
  return __builtin_bit_cast(float, __builtin_amdgcn_mov_dpp(__builtin_bit_cast(int, x), CTRL, 0xf, 0xf, true));
}
template <int CTRL> __device__ __forceinline__ unsigned dppu(unsigned x) {
  return (unsigned)__builtin_amdgcn_mov_dpp((int)x, CTRL, 0xf, 0xf, true);
}
constexpr int ROR1 = 0x121, ROR2 = 0x122, ROR4 = 0x124, ROR8 = 0x128, XOR1 = 0xB1, XOR2 = 0x4E, XOR7 = 0x141;
__device__ __forceinline__ float x16_sum(float x) {
  auto s = __builtin_amdgcn_permlane16_swap(__float_as_uint(x), __float_as_uint(x), false, false);
  return __uint_as_float(s[0]) + __uint_as_float(s[1]);
}
__device__ __forceinline__ float x32_sum(float x) {
  auto s = __builtin_amdgcn_permlane32_swap(__float_as_uint(x), __float_as_uint(x), false, false);
  return __uint_as_float(s[0]) + __uint_as_float(s[1]);
}
__device__ __forceinline__ float x16_max(float x) {
  auto s = __builtin_amdgcn_permlane16_swap(__float_as_uint(x), __float_as_uint(x), false, false);
  return fmaxf(__uint_as_float(s[0]), __uint_as_float(s[1]));
}
__device__ __forceinline__ float x32_max(float x) {
  auto s = __builtin_amdgcn_permlane32_swap(__float_as_uint(x), __float_as_uint(x), false, false);
  return fmaxf(__uint_as_float(s[0]), __uint_as_float(s[1]));
}
__device__ __forceinline__ float rs32(float a, float b) {
  auto s = __builtin_amdgcn_permlane32_swap(__float_as_uint(a), __float_as_uint(b), false, false);
  return __uint_as_float(s[0]) + __uint_as_float(s[1]);
}
__device__ __forceinline__ float rs16(float a, float b) {
  auto s = __builtin_amdgcn_permlane16_swap(__float_as_uint(a), __float_as_uint(b), false, false);
  return __uint_as_float(s[0]) + __uint_as_float(s[1]);
}
__device__ __forceinline__ float row_sum16(float x) {
  x += dpp<ROR8>(x); x += dpp<ROR4>(x); x += dpp<ROR2>(x); x += dpp<ROR1>(x); return x;
}
__device__ __forceinline__ unsigned perm_lo(unsigned a, unsigned b) { return __builtin_amdgcn_perm(b, a, 0x05040100u); }
__device__ __forceinline__ unsigned perm_hi(unsigned a, unsigned b) { return __builtin_amdgcn_perm(b, a, 0x07060302u); }
__device__ __forceinline__ u32x4 ldnt(const void* p) { return __builtin_nontemporal_load((const GAS u32x4*)p); }
__device__ __forceinline__ u32x4 ldpl(const void* p) { return *(const GAS u32x4*)p; }
__device__ __forceinline__ unsigned mix32(unsigned h) {
  h ^= h >> 16; h *= 0x85ebca6bu; h ^= h >> 13; h *= 0xc2b2ae35u; h ^= h >> 16; return h;
}
__device__ __forceinline__ float frcp(float x) { return __builtin_amdgcn_rcpf(x); }
__device__ __forceinline__ float ex2(float x) { return __builtin_amdgcn_exp2f(x); }
#define SB() __builtin_amdgcn_sched_barrier(0)
#define CFENCE() asm volatile("" ::: "memory")
#define PIN2(a, b) asm volatile("" : "+v"(a), "+v"(b) :: "memory")
#define PIN4(a) asm volatile("" : "+v"(a[0]), "+v"(a[1]), "+v"(a[2]), "+v"(a[3]) :: "memory")
#define PIN8(a) asm volatile("" : "+v"(a[0]), "+v"(a[1]), "+v"(a[2]), "+v"(a[3]), "+v"(a[4]), "+v"(a[5]), "+v"(a[6]), "+v"(a[7]) :: "memory")
#define ST16(v, off) __builtin_amdgcn_raw_buffer_store_b128((v), rs, (int)(off), 0, AUX_SC1)
#define ST8(v, off) __builtin_amdgcn_raw_buffer_store_b64((v), rs, (int)(off), 0, AUX_SC1)
#define LD16(off) __builtin_amdgcn_raw_buffer_load_b128(rs, (int)(off), 0, AUX_SC1)
#define LD8(off) __builtin_amdgcn_raw_buffer_load_b64(rs, (int)(off), 0, AUX_SC1)

constexpr float LOG2E = 1.4426950408889634f;
constexpr float SCALE = 0.08838834764831845f;

__device__ __forceinline__ void quad_rs(const float (&y)[8], int lane, float& u0, float& u1) {
  const bool b1 = lane & 2, b0 = lane & 1;
  float t[4];
#pragma unroll
  for (int j = 0; j < 4; ++j) t[j] = (b1 ? y[j + 4] : y[j]) + dpp<XOR2>(b1 ? y[j] : y[j + 4]);
  u0 = (b0 ? t[2] : t[0]) + dpp<XOR1>(b0 ? t[0] : t[2]);
  u1 = (b0 ? t[3] : t[1]) + dpp<XOR1>(b0 ? t[1] : t[3]);
}
__device__ __forceinline__ void ln_consts(float mean, float var, float& mb, float& r) {
  mb = rbf(mean);
  r = frcp(rbf(__builtin_amdgcn_sqrtf(rbf(rbf(var) + 1e-5f))));
}
__device__ __forceinline__ unsigned ln_pair(unsigned raw, float mb, float r) {
  const unsigned n = cvtpk(lo2f(raw) - mb, hi2f(raw) - mb);
  return cvtpk(lo2f(n) * r, hi2f(n) * r);
}

constexpr int L_BIG = 0;
constexpr int L_IMG = 0;
constexpr int L_XN = 131072;
constexpr int L_Q = L_XN + 2048;
constexpr int L_OW = L_Q + 256;
constexpr int L_ML = L_OW + 2048;
constexpr int L_OP = L_ML + 64;
constexpr int L_ST = L_OP + 1024;
constexpr int L_GP = L_ST + 64;
constexpr int L_RED = L_GP + 512;
constexpr int L_ROLE = L_RED + 512;
constexpr int L_GO = L_ROLE + 16;
constexpr int LDS_BYTES = L_ROLE + 64;

struct Tile { u32x4 k[4]; u32x4 v[4]; };

__global__ __launch_bounds__(NT, 2) void fused_kernel(Params p) {
  extern __shared__ __attribute__((aligned(16))) unsigned char lds[];
  const int tid = threadIdx.x, lane = tid & 63, wv = __builtin_amdgcn_readfirstlane(tid >> 6), w = wv - 1;
  const int bid = blockIdx.x;
  const int xcd = bid & 7, jj = bid >> 3;
  const int h = xcd * 4 + (jj >> 3), b = (jj >> 2) & 1, sp = jj & 3;
  const int lid = (b * H + h) * 4 + sp;
  const int r = lane >> 4, c = lane & 15;
  const __amdgpu_buffer_rsrc_t rs = __builtin_amdgcn_make_buffer_rsrc((void*)p.ws, 0, (int)WS_USED, 0x00020000);
#define READ_WCNT() ({ size_t wa_ = (size_t)(p.ws + WS_W); asm volatile("" : "+s"(wa_)); unsigned v_ = *(const CAS unsigned*)wa_; asm volatile("" : "+s"(v_)); v_; })
  if (wv == 0) {
    __builtin_amdgcn_s_setprio(3);
    const int u = 4 * b + sp, ch = u & 1, kq = u >> 1;
    const u32x4 xraw0 = ldpl(p.x + lane * 8), xraw1 = ldpl(p.x + D + lane * 8);
    CFENCE(); SB();
    u32x4 wq[16];
    const unsigned char* q_ = (const unsigned char*)p.wq + ((size_t)(128 * kq + 16 * (lane >> 3)) * (H * DH) + h * DH + 64 * ch + 8 * (lane & 7)) * 2;
#pragma unroll
    for (int i = 0; i < 16; ++i) wq[i] = ldnt(q_ + (size_t)i * (H * DH * 2));
    *(volatile LAS unsigned*)(size_t)(unsigned)L_GO = 0u;
    CFENCE(); SB();
    __syncthreads();
    SB();
    const unsigned wcnt = READ_WCNT();
    const unsigned E = mix32(wcnt + 1u);
    if (lane == 0 && bid == 0) ST8((u32x2{0u, E}), WS_TK);
    {
      float v0[8], v1[8];
#pragma unroll
      for (int i = 0; i < 4; ++i) { v0[2 * i] = lo2f(xraw0[i]); v0[2 * i + 1] = hi2f(xraw0[i]); v1[2 * i] = lo2f(xraw1[i]); v1[2 * i + 1] = hi2f(xraw1[i]); }
      const float c0 = __uint_as_float(__builtin_amdgcn_readfirstlane(__float_as_uint(v0[0])));
      const float c1 = __uint_as_float(__builtin_amdgcn_readfirstlane(__float_as_uint(v1[0])));
      float s0 = 0.f, q0 = 0.f, s1 = 0.f, q1 = 0.f;
#pragma unroll
      for (int i = 0; i < 8; ++i) { const float d0 = v0[i] - c0, d1 = v1[i] - c1; s0 += d0; q0 = fmaf(d0, d0, q0); s1 += d1; q1 = fmaf(d1, d1, q1); }
      float s = rs32(s0, s1), q = rs32(q0, q1);
      s = row_sum16(x16_sum(s)); q = row_sum16(x16_sum(q));
      const float ms = s * (1.0f / D);
      const float var = fmaxf(q * (1.0f / D) - ms * ms, 0.f);
      auto sm_ = __builtin_amdgcn_permlane32_swap(__float_as_uint(ms), __float_as_uint(ms), false, false);
      auto sv_ = __builtin_amdgcn_permlane32_swap(__float_as_uint(var), __float_as_uint(var), false, false);
      float mb0, rr0, mb1, rr1;
      ln_consts(c0 + __uint_as_float(sm_[0]), __uint_as_float(sv_[0]), mb0, rr0);
      ln_consts(c1 + __uint_as_float(sm_[1]), __uint_as_float(sv_[1]), mb1, rr1);
      u32x4* xs = (u32x4*)(lds + L_XN);
      xs[lane] = u32x4{ln_pair(xraw0[0], mb0, rr0), ln_pair(xraw0[1], mb0, rr0), ln_pair(xraw0[2], mb0, rr0), ln_pair(xraw0[3], mb0, rr0)};
      xs[64 + lane] = u32x4{ln_pair(xraw1[0], mb1, rr1), ln_pair(xraw1[1], mb1, rr1), ln_pair(xraw1[2], mb1, rr1), ln_pair(xraw1[3], mb1, rr1)};
    }
    SB();
#pragma unroll
    for (int i = 0; i < 16; i += 8)
      asm volatile("" : "+v"(wq[i]), "+v"(wq[i + 1]), "+v"(wq[i + 2]), "+v"(wq[i + 3]), "+v"(wq[i + 4]), "+v"(wq[i + 5]), "+v"(wq[i + 6]), "+v"(wq[i + 7]));
    {
      const u32x4* xs = (const u32x4*)(lds + L_XN) + 16 * kq + 2 * (lane >> 3);
      const u32x4 xa0 = xs[0], xb0 = xs[1], xa1 = xs[64], xb1 = xs[65];
      float a0[8] = {0, 0, 0, 0, 0, 0, 0, 0}, a1[8] = {0, 0, 0, 0, 0, 0, 0, 0};
#pragma unroll
      for (int pp = 0; pp < 8; ++pp) {
        const unsigned x0 = pp < 4 ? xa0[pp] : xb0[pp - 4], x1 = pp < 4 ? xa1[pp] : xb1[pp - 4];
#pragma unroll
        for (int k = 0; k < 4; ++k) {
          const unsigned lo = perm_lo(wq[2 * pp][k], wq[2 * pp + 1][k]), hi = perm_hi(wq[2 * pp][k], wq[2 * pp + 1][k]);
          a0[2 * k] = dot2(lo, x0, a0[2 * k]); a0[2 * k + 1] = dot2(hi, x0, a0[2 * k + 1]);
          a1[2 * k] = dot2(lo, x1, a1[2 * k]); a1[2 * k + 1] = dot2(hi, x1, a1[2 * k + 1]);
        }
      }
      float t[8], sv[4];
#pragma unroll
      for (int j = 0; j < 8; ++j) t[j] = rs32(a0[j], a1[j]);
#pragma unroll
      for (int j = 0; j < 4; ++j) sv[j] = rs16(t[j], t[j + 4]);
      const bool b3 = (lane >> 3) & 1;
      const float u0 = (b3 ? sv[2] : sv[0]) + dpp<ROR8>(b3 ? sv[0] : sv[2]), u1 = (b3 ? sv[3] : sv[1]) + dpp<ROR8>(b3 ? sv[1] : sv[3]);
      const int tt = lane & 31, src = ((lane & 32) + 16 * ((tt >> 1) & 1) + 8 * (tt & 1) + (tt >> 2)) * 4;
      const unsigned w0 = (unsigned)__builtin_amdgcn_ds_bpermute(src, (int)__float_as_uint(u0)), w1 = (unsigned)__builtin_amdgcn_ds_bpermute(src, (int)__float_as_uint(u1));
      ST16((u32x4{w0, E, w1, E}), WS_QG + (unsigned)((((h * 2 + (lane >> 5)) * 4 + kq) * 128 + 64 * ch + 2 * tt) * 8));
    }
    u32x2 g;
    {
      const unsigned qo = WS_QG + (unsigned)((((h * 2 + b) * 4) * 128 + 2 * lane) * 8);
      u32x4 g0, g1, g2, g3;
      for (unsigned spins = 0; spins < (SPIN_CAP << 4) && *(volatile LAS unsigned*)(size_t)(unsigned)L_GO == 0u; ++spins) __builtin_amdgcn_s_sleep(4);
      for (unsigned spins = 0; spins < SPIN_CAP; ++spins) {
        if (spins) __builtin_amdgcn_s_sleep(16);
        CFENCE();
        g0 = LD16(qo); g1 = LD16(qo + 1024); g2 = LD16(qo + 2048); g3 = LD16(qo + 3072);
        if (__all(g0[1] == E && g0[3] == E && g1[1] == E && g1[3] == E && g2[1] == E && g2[3] == E && g3[1] == E && g3[3] == E)) break;
      }
      g[0] = cvtpk((__uint_as_float(g0[0]) + __uint_as_float(g1[0])) + (__uint_as_float(g2[0]) + __uint_as_float(g3[0])),
                   (__uint_as_float(g0[2]) + __uint_as_float(g1[2])) + (__uint_as_float(g2[2]) + __uint_as_float(g3[2])));
    }
    ((unsigned*)(lds + L_Q))[lane] = g[0];
    __syncthreads();
    return;
  }
  const int rot = (lid & 7) * 4;
  const unsigned char* kbase = (const unsigned char*)p.kc + ((size_t)lid * 2048 * DH + (size_t)w * 16 * DH) * 2 + lane * 16;
  const unsigned char* vbase = (const unsigned char*)p.vc + ((size_t)lid * 2048 * DH + (size_t)w * 16 * DH) * 2 + lane * 16;
#define TOFF(i) ((size_t)((((i) + rot) & 31)) * (4 * 16 * DH * 2))
#define LOADK(T, i) do { const unsigned char* q_ = kbase + TOFF(i); _Pragma("unroll") for (int t_ = 0; t_ < 4; ++t_) T.k[t_] = ldnt(q_ + t_ * 1024); CFENCE(); SB(); } while (0)
#define LOADV(T, i) do { const unsigned char* q_ = vbase + TOFF(i); _Pragma("unroll") for (int t_ = 0; t_ < 4; ++t_) T.v[t_] = ldnt(q_ + t_ * 1024); CFENCE(); SB(); } while (0)
  Tile T0, T1, T2, T3;
  SB();
  __syncthreads();
  SB();
#define PACE() asm volatile("s_waitcnt vmcnt(12)" ::: "memory")
  LOADK(T0, 0); LOADV(T0, 0); LOADK(T1, 1); LOADV(T1, 1); PACE(); LOADK(T2, 2); PACE(); LOADV(T2, 2); PACE(); LOADK(T3, 3); PACE(); LOADV(T3, 3);
  {
    unsigned keep;
    asm volatile("s_mov_b32 %0, m0" : "=s"(keep));
#pragma unroll
    for (int j = 0; j < 4; ++j) {
      const unsigned char* kp = kbase + TOFF(4 + j);
      const unsigned char* vp = vbase + TOFF(4 + j);
      PACE();
#pragma unroll
      for (int t = 0; t < 4; ++t)
        asm volatile("s_mov_b32 m0, %1\n\ts_nop 0\n\tglobal_load_lds_dwordx4 %0, off nt" :: "v"(kp + t * 1024), "s"(L_BIG + w * 32768 + j * 8192 + t * 1024) : "memory");
      PACE();
#pragma unroll
      for (int t = 0; t < 4; ++t)
        asm volatile("s_mov_b32 m0, %1\n\ts_nop 0\n\tglobal_load_lds_dwordx4 %0, off nt" :: "v"(vp + t * 1024), "s"(L_BIG + w * 32768 + j * 8192 + 4096 + t * 1024) : "memory");
      if (j == 1 && w == 0) *(volatile LAS unsigned*)(size_t)(unsigned)L_GO = 1u;
    }
    asm volatile("s_mov_b32 m0, %0" :: "s"(keep));
  }
  const unsigned wcnt = READ_WCNT();
  const unsigned E = mix32(wcnt + 1u);
  const unsigned T16 = (((wcnt + 1u) * 40503u) & 0xffffu) << 16;
  __syncthreads();
  const u32x4 q4 = *(const u32x4*)(lds + L_Q + c * 16);
  float m = -INFINITY, l = 0.f;
  float acc[8] = {0, 0, 0, 0, 0, 0, 0, 0};
  auto qk = [&](const Tile& T, float (&s)[4]) {
#pragma unroll
    for (int t = 0; t < 4; ++t) {
      float d = dot2(T.k[t][0], q4[0], 0.f);
      d = dot2(T.k[t][1], q4[1], d); d = dot2(T.k[t][2], q4[2], d); d = dot2(T.k[t][3], q4[3], d);
      s[t] = d;
    }
  };
  auto sm = [&](float (&s)[4]) {
#pragma unroll
    for (int t = 0; t < 4; ++t) s[t] = row_sum16(s[t]);
    const unsigned a01 = cvtpk(s[0], s[1]), a23 = cvtpk(s[2], s[3]);
    const unsigned b01 = cvtpk(lo2f(a01) * SCALE, hi2f(a01) * SCALE), b23 = cvtpk(lo2f(a23) * SCALE, hi2f(a23) * SCALE);
    const float z0 = lo2f(b01) * LOG2E, z1 = hi2f(b01) * LOG2E, z2 = lo2f(b23) * LOG2E, z3 = hi2f(b23) * LOG2E;
    const float mn = fmaxf(fmaxf(fmaxf(z0, z1), fmaxf(z2, z3)), m);
    const float al = ex2(m - mn);
    s[0] = ex2(z0 - mn); s[1] = ex2(z1 - mn); s[2] = ex2(z2 - mn); s[3] = ex2(z3 - mn);
    l = fmaf(l, al, (s[0] + s[1]) + (s[2] + s[3]));
    m = mn;
#pragma unroll
    for (int k = 0; k < 8; ++k) acc[k] *= al;
  };
  auto pv = [&](const Tile& T, const float (&s)[4]) {
#pragma unroll
    for (int t = 0; t < 4; ++t)
#pragma unroll
      for (int k = 0; k < 4; ++k) {
        acc[2 * k] = fmaf(s[t], lo2f(T.v[t][k]), acc[2 * k]);
        acc[2 * k + 1] = fmaf(s[t], hi2f(T.v[t][k]), acc[2 * k + 1]);
      }
  };
#define STEP(T, i_next) do { float s_[4]; qk(T, s_); PIN4(s_); SB(); LOADK(T, i_next); sm(s_); pv(T, s_); PIN8(acc); SB(); LOADV(T, i_next); } while (0)
#define STEP_LAST(T) do { float s_[4]; qk(T, s_); sm(s_); pv(T, s_); PIN8(acc); SB(); } while (0)
  {
    const u32x4* tl = (const u32x4*)(lds + L_BIG + w * 32768) + lane;
#define STEP_LDS(j) do { asm volatile("s_waitcnt vmcnt(32)" ::: "memory"); Tile TL; \
      _Pragma("unroll") for (int t_ = 0; t_ < 4; ++t_) { TL.k[t_] = tl[(j * 8 + t_) * 64]; TL.v[t_] = tl[(j * 8 + 4 + t_) * 64]; } \
      float s_[4]; qk(TL, s_); sm(s_); pv(TL, s_); PIN8(acc); SB(); } while (0)
    STEP(T0, 8); STEP_LDS(0); STEP(T1, 9); STEP_LDS(1); STEP(T2, 10); STEP_LDS(2); STEP(T3, 11); STEP_LDS(3);
  }
  {
    const unsigned char* wob = (const unsigned char*)p.wo + ((size_t)(h * DH + 32 * (lane & 3)) * D + 128 * w + 8 * (lane >> 2)) * 2;
    u32x4* wl = (u32x4*)(lds + L_BIG + w * 32768) + lane;
    for (int it = 0; it < 5; ++it) {
      u32x4 wo[8];
      const int ch = it < 4 ? it : 3;
#pragma unroll
      for (int i = 0; i < 8; ++i) wo[i] = ldnt(it < 4 ? wob + (size_t)(8 * ch + i) * (D * 2) : (const unsigned char*)p.wo);
      CFENCE(); SB();
      STEP(T0, 4 * it + 12); STEP(T1, 4 * it + 13); STEP(T2, 4 * it + 14); STEP(T3, 4 * it + 15);
#pragma unroll
      for (int pp = 0; pp < 4; ++pp) {
        u32x4 lo, hi;
#pragma unroll
        for (int k = 0; k < 4; ++k) { lo[k] = perm_lo(wo[2 * pp][k], wo[2 * pp + 1][k]); hi[k] = perm_hi(wo[2 * pp][k], wo[2 * pp + 1][k]); }
        if (it < 4) { wl[(8 * ch + 2 * pp) * 64] = lo; wl[(8 * ch + 2 * pp + 1) * 64] = hi; }
      }
      SB();
    }
  }
  const bool is_red = bid < NRED;
  const int rb = (bid >> 5) & 1, blk = bid & 31;
  unsigned yoff = WS_YG + (unsigned)((((b * 32 + 8 * w + (lane >> 3)) * 4 + sp) * 32 + h) * 128 + (lane & 7) * 16);
  unsigned zoff = WS_ZG + (unsigned)((8 * w + (lane >> 3)) * 88 * 128 + (lane & 7) * 16);
  PIN2(yoff, zoff);
  u32x4 wop[32];
  const u32x4* wlr = (const u32x4*)(lds + L_BIG + w * 32768) + lane;
  STEP_LAST(T0);
#pragma unroll
  for (int i = 0; i < 8; ++i) wop[i] = wlr[i * 64];
  SB();
  STEP_LAST(T1);
#pragma unroll
  for (int i = 8; i < 16; ++i) wop[i] = wlr[i * 64];
  SB();
  STEP_LAST(T2);
#pragma unroll
  for (int i = 16; i < 24; ++i) wop[i] = wlr[i * 64];
  SB();
  STEP_LAST(T3);
#pragma unroll
  for (int i = 24; i < 32; ++i) wop[i] = wlr[i * 64];
  {
    const float mw = x32_max(x16_max(m));
    const float e = ex2(m - mw);
    const float lw = x32_sum(x16_sum(l * e));
    float a[8];
#pragma unroll
    for (int k = 0; k < 8; ++k) a[k] = acc[k] * e;
    const float t0 = rs32(a[0], a[4]), t1 = rs32(a[1], a[5]), t2 = rs32(a[2], a[6]), t3 = rs32(a[3], a[7]);
    const float u0 = rs16(t0, t2), u1 = rs16(t1, t3);
    float* ow = (float*)(lds + L_OW) + w * 128 + c * 8 + (r >> 1) * 4 + (r & 1) * 2;
    *(float2*)ow = make_float2(u0, u1);
    if (lane == 0) { ((float*)(lds + L_ML))[w] = mw; ((float*)(lds + L_ML))[4 + w] = lw; }
  }
  __syncthreads();
  {
    const f32x4 m4 = *(const f32x4*)(lds + L_ML), l4 = *(const f32x4*)(lds + L_ML + 16);
    const float M = fmaxf(fmaxf(m4[0], m4[1]), fmaxf(m4[2], m4[3]));
    float L = 0.f, o0 = 0.f, o1 = 0.f;
#pragma unroll
    for (int ww = 0; ww < 4; ++ww) {
      const float e = ex2(m4[ww] - M);
      L = fmaf(e, l4[ww], L);
      const float2 v = *(const float2*)((const float*)(lds + L_OW) + ww * 128 + 2 * lane);
      o0 = fmaf(e, v.x, o0); o1 = fmaf(e, v.y, o1);
    }
    const float inv = frcp(L);
    unsigned* op = (unsigned*)(lds + L_OP) + w * 64;
    op[lane] = cvtpk(o0 * inv, o1 * inv);
    const unsigned lse = __float_as_uint(M + __builtin_amdgcn_logf(L));
    u32x4 ov[4];
#pragma unroll
    for (int i = 0; i < 4; ++i) ov[i] = *(const u32x4*)(op + 16 * (lane & 3) + 4 * i);
    float y[8] = {0, 0, 0, 0, 0, 0, 0, 0};
#pragma unroll
    for (int pp = 0; pp < 16; ++pp) {
      const unsigned o2 = ov[pp >> 2][pp & 3];
#pragma unroll
      for (int k = 0; k < 4; ++k) {
        y[2 * k] = dot2(wop[2 * pp][k], o2, y[2 * k]);
        y[2 * k + 1] = dot2(wop[2 * pp + 1][k], o2, y[2 * k + 1]);
      }
    }
    float u0, u1;
    quad_rs(y, lane, u0, u1);
    ST16((u32x4{__float_as_uint(u0), T16 | (lse >> 16), __float_as_uint(u1), T16 | (lse & 0xffffu)}), yoff);
  }
  if (is_red) {
    const unsigned xres2 = *(const GAS unsigned*)(p.x + rb * D + 16 * blk + 2 * (lane & 7));
    u32x4 yv[4];
    const unsigned yo = WS_YG + (unsigned)((((rb * 32 + blk) * 4) * 32 + 8 * w) * 128 + lane * 16);
#define H1_LOAD(Y) do { _Pragma("unroll") for (int s_ = 0; s_ < 4; ++s_) Y[s_] = LD16(yo + s_ * 4096); } while (0)
#define H1_BAD(Y) (((Y[0][1] ^ T16) | (Y[0][3] ^ T16) | (Y[1][1] ^ T16) | (Y[1][3] ^ T16) | (Y[2][1] ^ T16) | (Y[2][3] ^ T16) | (Y[3][1] ^ T16) | (Y[3][3] ^ T16)) >> 16)
    for (unsigned spins = 0; spins < SPIN_CAP; ++spins) {
      CFENCE();
      H1_LOAD(yv);
      const u64 pend = __ballot(H1_BAD(yv) != 0u);
      if (pend == 0) break;
      if (__builtin_popcountll(pend) > 16) __builtin_amdgcn_s_sleep(8);
    }
    {
      float ls[4];
#pragma unroll
      for (int s_ = 0; s_ < 4; ++s_) ls[s_] = __uint_as_float((yv[s_][1] << 16) | (yv[s_][3] & 0xffffu));
      const float mx = fmaxf(fmaxf(ls[0], ls[1]), fmaxf(ls[2], ls[3]));
      const float e0 = ex2(ls[0] - mx), e1 = ex2(ls[1] - mx), e2 = ex2(ls[2] - mx), e3 = ex2(ls[3] - mx);
      const float inv = frcp((e0 + e1) + (e2 + e3));
      const float v0 = inv * fmaf(e3, __uint_as_float(yv[3][0]), fmaf(e2, __uint_as_float(yv[2][0]), fmaf(e1, __uint_as_float(yv[1][0]), e0 * __uint_as_float(yv[0][0]))));
      const float v1 = inv * fmaf(e3, __uint_as_float(yv[3][2]), fmaf(e2, __uint_as_float(yv[2][2]), fmaf(e1, __uint_as_float(yv[1][2]), e0 * __uint_as_float(yv[0][2]))));
      float val = x16_sum(rs32(v0, v1));
      val += dpp<ROR8>(val);
      if ((lane & 0x18) == 0) ((float*)(lds + L_RED))[w * 16 + 2 * (lane & 7) + (lane >> 5)] = val;
    }
    __syncthreads();
    float h0 = 0.f, h1 = 0.f;
    if (w == 0) {
      const float2* rp = (const float2*)(lds + L_RED) + (lane & 7);
      const float2 a0 = rp[0], a1 = rp[8], a2 = rp[16], a3 = rp[24];
      h0 = rbf(lo2f(xres2) + rbf((a0.x + a1.x) + (a2.x + a3.x)));
      h1 = rbf(hi2f(xres2) + rbf((a0.y + a1.y) + (a2.y + a3.y)));
      ST16((u32x4{__float_as_uint(h0), E, __float_as_uint(h1), E}), WS_HG + (unsigned)((((lane >> 3) * 2 + rb) * 512 + 16 * blk + 2 * (lane & 7)) * 8));
    }
    __builtin_amdgcn_s_sleep(40);
    {
      const unsigned zo = WS_ZG + (unsigned)(((rb * 32 + blk) * 88 + 8 * w) * 128 + lane * 16);
      const bool live2 = w < 2 || (w == 2 && lane < 48);
      const unsigned zo2 = live2 ? zo + 8192 : zo;
      u32x4 g0, g1, g2;
      for (unsigned spins = 0; spins < SPIN_CAP; ++spins) {
        CFENCE();
        g0 = LD16(zo); g1 = LD16(zo + 4096); g2 = LD16(zo2);
        if (__all(g0[1] == E && g0[3] == E && g1[1] == E && g1[3] == E && g2[1] == E && g2[3] == E)) break;
      }
      const float z0 = __uint_as_float(g0[0]) + __uint_as_float(g1[0]) + (live2 ? __uint_as_float(g2[0]) : 0.f);
      const float z1 = __uint_as_float(g0[2]) + __uint_as_float(g1[2]) + (live2 ? __uint_as_float(g2[2]) : 0.f);
      float zs = x16_sum(rs32(z0, z1));
      zs += dpp<ROR8>(zs);
      if ((lane & 0x18) == 0) ((float*)(lds + L_RED))[64 + w * 16 + 2 * (lane & 7) + (lane >> 5)] = zs;
    }
    __syncthreads();
    if (w == 0) {
      const float2* rp = (const float2*)(lds + L_RED + 256) + (lane & 7);
      const float2 a0 = rp[0], a1 = rp[8], a2 = rp[16], a3 = rp[24];
      const unsigned o2 = cvtpk(h0 + rbf((a0.x + a1.x) + (a2.x + a3.x)), h1 + rbf((a0.y + a1.y) + (a2.y + a3.y)));
      if (lane < 8) *(GAS unsigned*)(p.out + rb * D + 16 * blk + 2 * lane) = o2;
      if (bid == 0 && lane == 0) __hip_atomic_store((gu32*)(p.ws + WS_W), wcnt + 1u, RLX_AGENT);
    }
    return;
  }
  volatile LAS unsigned* role = (volatile LAS unsigned*)(size_t)(unsigned)L_ROLE;
  if (w == 0 && lane == 0) {
    unsigned tk = NFFN;
    for (unsigned spins = 0; spins < SPIN_CAP; ++spins) {
      const u64 old = __hip_atomic_fetch_add((gu64*)(p.ws + WS_TK), 1ull, RLX_AGENT);
      tk = (unsigned)old;
      if ((unsigned)(old >> 32) == E) break;
      tk = NFFN;
      for (; spins < SPIN_CAP && (unsigned)(__hip_atomic_load((gu64*)(p.ws + WS_TK), RLX_AGENT) >> 32) != E; ++spins) __builtin_amdgcn_s_sleep(2);
    }
    *role = tk;
  }
  __syncthreads();
  const int fj = __builtin_amdgcn_readfirstlane(*role);
  if (fj >= NFFN) return;
  {
    u32x4 wdp[4], bfr[8];
    {
      const unsigned short* src = ((w & 2) ? p.wu : p.wg) + (size_t)(256 * (w & 1) + (lane >> 1)) * F + 16 * fj + 8 * (lane & 1);
      unsigned keep;
      asm volatile("s_mov_b32 %0, m0" : "=s"(keep));
#pragma unroll
      for (int i = 0; i < 8; ++i)
        asm volatile("s_mov_b32 m0, %1\n\ts_nop 0\n\tglobal_load_lds_dwordx4 %0, off nt" :: "v"(src + (size_t)i * 32 * F), "s"(L_IMG + (8 * w + i) * 1024) : "memory");
      asm volatile("s_mov_b32 m0, %0" :: "s"(keep));
      const unsigned short* dp = p.wd + (size_t)(16 * fj + 2 * (lane & 3)) * D + 128 * w + 8 * (lane >> 2);
      const u32x4 w0 = ldnt(dp), w1 = ldnt(dp + D), w2 = ldnt(dp + 8 * D), w3 = ldnt(dp + 9 * D);
      asm volatile("s_waitcnt vmcnt(4)" ::: "memory");
      __syncthreads();
      const unsigned base = (unsigned)(L_IMG + (w & 1) * 16384 + (256 * (w >> 1) + 8 * r + ((lane & 15) >> 2)) * 32 + (lane & 3) * 8);
#pragma unroll
      for (int ks = 0; ks < 8; ++ks) {
        const s16x4 lo = __builtin_amdgcn_ds_read_tr16_b64_v4i16((LAS s16x4*)(size_t)(base + ks * 1024));
        const s16x4 hi = __builtin_amdgcn_ds_read_tr16_b64_v4i16((LAS s16x4*)(size_t)(base + ks * 1024 + 128));
        const u32x2 l2 = __builtin_bit_cast(u32x2, lo), h2 = __builtin_bit_cast(u32x2, hi);
        bfr[ks] = u32x4{l2[0], l2[1], h2[0], h2[1]};
      }
#pragma unroll
      for (int k = 0; k < 4; ++k) {
        wdp[0][k] = perm_lo(w0[k], w1[k]); wdp[1][k] = perm_hi(w0[k], w1[k]);
        wdp[2][k] = perm_lo(w2[k], w3[k]); wdp[3][k] = perm_hi(w2[k], w3[k]);
      }
    }
    zoff += (unsigned)fj * 128;
    u32x4 ha, hb;
    {
      const unsigned ho = WS_HG + (unsigned)((((bid & (REP_H - 1)) * 2 + (w >> 1)) * 512 + 256 * (w & 1) + 2 * lane) * 8);
      for (unsigned spins = 0; spins < SPIN_CAP; ++spins) {
        CFENCE();
        ha = LD16(ho); hb = LD16(ho + 1024);
        const u64 pend = __ballot(!(ha[1] == E && ha[3] == E && hb[1] == E && hb[3] == E));
        if (pend == 0) break;
        if (pend == ~0ull) __builtin_amdgcn_s_sleep(6);
      }
    }
    {
      const float v0 = __uint_as_float(ha[0]), v1 = __uint_as_float(ha[2]), v2 = __uint_as_float(hb[0]), v3 = __uint_as_float(hb[2]);
      const float c0 = __uint_as_float(__builtin_amdgcn_readfirstlane(ha[0]));
      const float d0 = v0 - c0, d1 = v1 - c0, d2 = v2 - c0, d3 = v3 - c0;
      float s = (d0 + d1) + (d2 + d3), q = fmaf(d0, d0, fmaf(d1, d1, fmaf(d2, d2, d3 * d3)));
      float sq = x16_sum(rs32(s, q));
      sq = row_sum16(sq);
      auto t2 = __builtin_amdgcn_permlane32_swap(__float_as_uint(sq), __float_as_uint(sq), false, false);
      const float S = __uint_as_float(t2[0]), Q = __uint_as_float(t2[1]);
      if (lane == 0) *(float2*)(lds + L_ST + w * 8) = make_float2(c0 + S * (1.0f / 256), Q - S * S * (1.0f / 256));
      __syncthreads();
      const f32x4 stt = *(const f32x4*)(lds + L_ST + (w >> 1) * 16);
      const float dm = stt[0] - stt[2];
      float mb, rr;
      ln_consts(0.5f * (stt[0] + stt[2]), fmaxf((stt[1] + stt[3] + 128.0f * dm * dm) * (1.0f / D), 0.f), mb, rr);
      const unsigned n0 = cvtpk(v0 - mb, v1 - mb), n1 = cvtpk(v2 - mb, v3 - mb);
      unsigned* xn = (unsigned*)(lds + L_XN) + 128 * w + lane;
      xn[0] = cvtpk(lo2f(n0) * rr, hi2f(n0) * rr); xn[64] = cvtpk(lo2f(n1) * rr, hi2f(n1) * rr);
    }
    __syncthreads();
    {
      const unsigned char* xa = lds + L_XN + ((lane & 1) * 256 + 128 * (w >> 1) + 4 * r) * 4;
      f32x4 d = {0.f, 0.f, 0.f, 0.f};
#pragma unroll
      for (int ks = 0; ks < 8; ++ks) {
        const u32x4 a = *(const u32x4*)(xa + ks * 64);
        d = __builtin_amdgcn_mfma_f32_16x16x32_bf16(__builtin_bit_cast(bf16x8, a), __builtin_bit_cast(bf16x8, bfr[ks]), d, 0, 0, 0);
      }
      if (lane < 16) *(float2*)(lds + L_GP + (w * 16 + lane) * 8) = make_float2(d[0], d[1]);
    }
    __syncthreads();
    unsigned a00, a01, a10, a11;
    {
      const float* gp = (const float*)(lds + L_GP) + (lane & 15) * 2 + ((lane >> 4) & 1);
      const float g = rbf(gp[0] + gp[64]), u = rbf(gp[32] + gp[96]);
      const float inner = 0.7978845608028654f * (g + 0.044715f * g * g * g);
      const float ex = ex2(inner * (2.0f * LOG2E));
      const float th = 1.0f - 2.0f * frcp(ex + 1.0f);
      const float ge = rbf(0.5f * g * (1.0f + th));
      const unsigned ab = cvtpk(ge * u, 0.f) & 0xffffu;
      const unsigned pr = ab | (dppu<XOR1>(ab) << 16);
      const int rg = lane & 3;
      a00 = __builtin_amdgcn_ds_bpermute((2 * rg) * 4, (int)pr); a01 = __builtin_amdgcn_ds_bpermute((2 * rg + 8) * 4, (int)pr);
      a10 = __builtin_amdgcn_ds_bpermute((16 + 2 * rg) * 4, (int)pr); a11 = __builtin_amdgcn_ds_bpermute((24 + 2 * rg) * 4, (int)pr);
    }
    float z0[8], z1[8];
#pragma unroll
    for (int k = 0; k < 4; ++k) {
      z0[2 * k] = dot2(wdp[2][k], a01, dot2(wdp[0][k], a00, 0.f)); z0[2 * k + 1] = dot2(wdp[3][k], a01, dot2(wdp[1][k], a00, 0.f));
      z1[2 * k] = dot2(wdp[2][k], a11, dot2(wdp[0][k], a10, 0.f)); z1[2 * k + 1] = dot2(wdp[3][k], a11, dot2(wdp[1][k], a10, 0.f));
    }
    float p00, p01, p10, p11;
    quad_rs(z0, lane, p00, p01); quad_rs(z1, lane, p10, p11);
    ST16((u32x4{__float_as_uint(p00), E, __float_as_uint(p01), E}), zoff);
    ST16((u32x4{__float_as_uint(p10), E, __float_as_uint(p11), E}), zoff + 32 * 88 * 128);
  }
}

extern "C" void kernel_launch(void* const* d_in, const int* in_sizes, int n_in, void* d_out, int out_size, void* d_ws, size_t ws_size,
                              hipStream_t stream) {
  Params p;
  p.x = (const unsigned short*)d_in[0]; p.kc = (const unsigned short*)d_in[1]; p.vc = (const unsigned short*)d_in[2];
  p.wq = (const unsigned short*)d_in[3]; p.wo = (const unsigned short*)d_in[4];
  p.wg = (const unsigned short*)d_in[6]; p.wu = (const unsigned short*)d_in[7]; p.wd = (const unsigned short*)d_in[8];
  p.out = (unsigned short*)d_out; p.ws = (unsigned char*)d_ws;
  (void)hipFuncSetAttribute((const void*)fused_kernel, hipFuncAttributeMaxDynamicSharedMemorySize, LDS_BYTES);
  hipLaunchKernelGGL(fused_kernel, dim3(NWG), dim3(NT), LDS_BYTES, stream, p);
}
```
